```python
import torch

def _softmax(x, axis=-1):
    return torch.softmax(x, dim=axis)
def _gelu(x):
    return torch.nn.functional.gelu(x, approximate="tanh")
def _silu(x):
    return torch.nn.functional.silu(x)
def _sigmoid(x):
    return torch.sigmoid(x)
def _layernorm(x, axis=-1, eps=1e-5):
    m = x.mean(dim=axis, keepdim=True)
    v = x.var(dim=axis, keepdim=True, unbiased=False)
    return (x - m) / torch.sqrt(v + eps)
def _scatter_add(tgt, idx, src, axis=0):
    return tgt.clone().index_add_(axis, idx, src)
def _segment_sum(src, seg, n):
    out = torch.zeros((n,) + src.shape[1:], dtype=src.dtype)
    return out.index_add_(0, seg, src)
def _pad(x, pw):
    flat = tuple(v for pair in reversed(pw) for v in pair)
    return torch.nn.functional.pad(x, flat)

kernel_name = 'sequential_preLN__sliding_block_cached__swiglu_ffn__decode'

def setup_inputs():

    _w = (lambda a: (lambda t: t.to(torch.bfloat16) if t.is_floating_point() else t)(torch.as_tensor(a)))
    def _A_setup():
        B, Sq, D = 4, 1, 512
        Hq, Hkv, Sk, Dh = 32, 32, 8192, 128
        assert Hq % Hkv == 0, (Hq, Hkv)
        return {
            "x": _w((lambda *s: torch.randn(*s, dtype=torch.bfloat16) * 0.05)(B, Sq, D)),
            "k_cache": _w((lambda *s: torch.randn(*s, dtype=torch.bfloat16) * 3.2)(B, Hkv, Sk, Dh)),
            "v_cache": _w((lambda *s: torch.randn(*s, dtype=torch.bfloat16) * 0.4)(B, Hkv, Sk, Dh)),
            "wq": _w((lambda *s: torch.randn(*s, dtype=torch.bfloat16) * 3.2)(D, Hq * Dh) * (D ** -0.5)),
            "wo": _w((lambda *s: torch.randn(*s, dtype=torch.bfloat16) * 0.4)(Hq * Dh, D) * ((Hq * Dh) ** -0.5)),
        }
    _A_in = _A_setup()

    def _B_setup():
        B, S, D, F = 4, 1, 512, 1376
        return {
            "x": _w((lambda *s: torch.randn(*s, dtype=torch.bfloat16) * 0.05)(B, S, D)),
            "w_gate": _w((lambda *s: torch.randn(*s, dtype=torch.bfloat16) * 0.05)(D, F)),
            "w_up": _w((lambda *s: torch.randn(*s, dtype=torch.bfloat16) * 0.05)(D, F)),
            "w_down": _w((lambda *s: torch.randn(*s, dtype=torch.bfloat16) * 0.05)(F, D)),
        }
    _B_in = _B_setup()

    return {
        "A_x": _A_in["x"],
        "A_k_cache": _A_in["k_cache"],
        "A_v_cache": _A_in["v_cache"],
        "A_wq": _A_in["wq"],
        "A_wo": _A_in["wo"],
        "B_x": _B_in["x"],
        "B_w_gate": _B_in["w_gate"],
        "B_w_up": _B_in["w_up"],
        "B_w_down": _B_in["w_down"],
    }

def reference(A_x, A_k_cache, A_v_cache, A_wq, A_wo, B_x, B_w_gate, B_w_up, B_w_down):
    def _A_ref(x, k_cache, v_cache, wq, wo):
        B, Sq, D = x.shape
        Hq, Dh = 32, 128
        Hkv, Sk = k_cache.shape[1], k_cache.shape[2]
        W = min(4096, max(256, Sk // 4))
        rep = Hq // Hkv
        q = (lambda x, *d: x.permute(*d))((x @ wq).reshape(B, Sq, Hq, Dh), 0, 2, 1, 3)
        k = (lambda x, n, axis: torch.repeat_interleave(x, n, dim=axis))(k_cache[:, :, Sk - W:, :], rep, axis=1)
        v = (lambda x, n, axis: torch.repeat_interleave(x, n, dim=axis))(v_cache[:, :, Sk - W:, :], rep, axis=1)
        scores = torch.einsum("bhqd,bhkd->bhqk", q, k) * (Dh ** -0.5)
        p = _softmax(scores, axis=-1)
        o = torch.einsum("bhqk,bhkd->bhqd", p, v)
        return (lambda x, *d: x.permute(*d))(o, 0, 2, 1, 3).reshape(B, Sq, Hq * Dh) @ wo

    def _B_ref(x, w_gate, w_up, w_down):
        g = _silu(x @ w_gate)
        u = x @ w_up
        return (g * u) @ w_down

    x = A_x
    h = x + _A_ref(_layernorm(x, axis=-1), A_k_cache, A_v_cache, A_wq, A_wo)
    h2 = h + _B_ref(_layernorm(h, axis=-1), B_w_gate, B_w_up, B_w_down)
    return h2
```

```cpp
#include <hip/hip_runtime.h>
#include <hip/hip_bf16.h>
#include <stdint.h>

typedef unsigned u32;
typedef unsigned long long u64;
typedef u32 u32x4 __attribute__((ext_vector_type(4)));
typedef float f32x4 __attribute__((ext_vector_type(4)));
typedef float f32x2 __attribute__((ext_vector_type(2)));
typedef __bf16 bf16x2 __attribute__((ext_vector_type(2)));
#define GAS __attribute__((address_space(1)))
typedef GAS u64 gu64;
#define RLX_AGENT __ATOMIC_RELAXED, __HIP_MEMORY_SCOPE_AGENT

constexpr int NB = 4, DM = 512, NH = 32, SK = 8192, DH = 128, WIN = 2048, FF = 1376, QD = NH * DH;
constexpr int NCG = FF / 32;
constexpr int NP = 64;
constexpr int NTHR = 1024;
constexpr float SCALE = 0.08838834764831845f;
constexpr float LOG2E = 1.4426950408889634f;
constexpr float EPS = 1e-5f;
constexpr int SPIN_CAP = 1 << 18;

constexpr size_t WS_EPOCH = 0;
constexpr size_t WS_ML = 128;
constexpr size_t WS_H = WS_ML + NB * NP * 2;
constexpr size_t WS_Y = WS_H + NB * DM;
constexpr size_t WS_Z = WS_Y + (size_t)NB * NP * DM;

constexpr int L_PART = 0;
constexpr int L_QBF = L_PART + 16 * 128;
constexpr int L_ML = L_QBF + 64;
constexpr int L_O = L_ML + 32;
constexpr int L_MLB = L_O + 128;
constexpr int L_H = L_MLB + 4;
constexpr int L_RED = L_H + 512;
constexpr int L_ACT = L_RED + 16 * 64;
constexpr int L_FFW = L_ACT + 32;
constexpr int L_TOTAL = L_FFW + 6 * 1024 * 4;
static_assert(L_FFW % 4 == 0 && L_H % 4 == 0 && L_RED % 4 == 0 && L_O % 4 == 0 && L_QBF % 4 == 0, "align");

__device__ __forceinline__ float rbf(float x) { return (float)(__bf16)x; }
__device__ __forceinline__ float blo(u32 u) { return __builtin_bit_cast(float, u << 16); }
__device__ __forceinline__ float bhi(u32 u) { return __builtin_bit_cast(float, u & 0xffff0000u); }
__device__ __forceinline__ float dot2(u32 a, u32 b, float acc) {
  return __builtin_amdgcn_fdot2_f32_bf16(__builtin_bit_cast(bf16x2, a), __builtin_bit_cast(bf16x2, b), acc, false);
}
template <int CTRL> __device__ __forceinline__ float dpp(float x) {
  return __builtin_bit_cast(float, __builtin_amdgcn_mov_dpp(__builtin_bit_cast(int, x), CTRL, 0xf, 0xf, true));
}
constexpr int XOR1 = 0xB1, XOR2 = 0x4E, XOR7 = 0x141, ROR8 = 0x128, ROR4 = 0x124;
__device__ __forceinline__ float tr32(float a, float b) {
  auto s = __builtin_amdgcn_permlane32_swap(__float_as_uint(a), __float_as_uint(b), false, false);
  return __uint_as_float(s[0]) + __uint_as_float(s[1]);
}
__device__ __forceinline__ float tr16(float a, float b) {
  auto s = __builtin_amdgcn_permlane16_swap(__float_as_uint(a), __float_as_uint(b), false, false);
  return __uint_as_float(s[0]) + __uint_as_float(s[1]);
}
__device__ __forceinline__ float wave_sum(float v) {
  v += dpp<XOR1>(v); v += dpp<XOR2>(v); v += dpp<XOR7>(v); v += dpp<ROR8>(v);
  v = tr16(v, v); v = tr32(v, v); return v;
}
__device__ __forceinline__ float wave_max(float v) {
  v = fmaxf(v, dpp<XOR1>(v)); v = fmaxf(v, dpp<XOR2>(v)); v = fmaxf(v, dpp<XOR7>(v)); v = fmaxf(v, dpp<ROR8>(v));
  auto s = __builtin_amdgcn_permlane16_swap(__float_as_uint(v), __float_as_uint(v), false, false);
  v = fmaxf(__uint_as_float(s[0]), __uint_as_float(s[1]));
  auto t = __builtin_amdgcn_permlane32_swap(__float_as_uint(v), __float_as_uint(v), false, false);
  return fmaxf(__uint_as_float(t[0]), __uint_as_float(t[1]));
}
__device__ __forceinline__ u32x4 ldnt(const void* base, u32 off) { return __builtin_nontemporal_load(reinterpret_cast<const u32x4*>((const char*)base + off)); }
__device__ __forceinline__ u32x4 ld16(const void* base, u32 off) { return *reinterpret_cast<const u32x4*>((const char*)base + off); }
#define SB() __builtin_amdgcn_sched_barrier(0)

__device__ __forceinline__ u64 gpack(u64 tag_hi, float v) {
  u32 u = __float_as_uint(v);
  u32 r = (u + 0x80u) >> 8;
  if ((u & 0x7f800000u) == 0x7f800000u) r = (u >> 8) | ((u & 0xffu) ? 1u : 0u);
  return tag_hi | (u64)(r & 0xffffffu);
}
__device__ __forceinline__ float gval(u64 g) { return __uint_as_float(((u32)g) << 8); }
__device__ __forceinline__ bool gok(u64 g, u64 tag_hi) { return ((g ^ tag_hi) >> 24) == 0; }
__device__ __forceinline__ void gstore(gu64* p, u64 v) { __hip_atomic_store(p, v, RLX_AGENT); }
__device__ __forceinline__ u64 gload(gu64* p) { return __hip_atomic_load(p, RLX_AGENT); }

__device__ __forceinline__ float red8_64(const float (&v)[8], int lane) {
  float a0 = tr32(v[0], v[4]), a1 = tr32(v[1], v[5]), a2 = tr32(v[2], v[6]), a3 = tr32(v[3], v[7]);
  float b0 = tr16(a0, a2), b1 = tr16(a1, a3);
  float t0 = b0 + dpp<ROR8>(b0), t1 = b1 + dpp<ROR8>(b1);
  float c = (lane & 8) ? t1 : t0;
  c += dpp<XOR7>(c); c += dpp<XOR2>(c); c += dpp<XOR1>(c);
  return c;
}

struct Params {
  const unsigned short* x; const unsigned short* kc; const unsigned short* vc; const unsigned short* wq; const unsigned short* wo;
  const unsigned short* wg; const unsigned short* wu; const unsigned short* wd; unsigned short* out; u64* ws;
};

__global__ void __launch_bounds__(NTHR, 4) fused_decode_block(Params P) {
  __shared__ __attribute__((aligned(16))) float lds[L_TOTAL];
  const int tid = threadIdx.x, lane = tid & 63, w = __builtin_amdgcn_readfirstlane(tid >> 6);
  const int r = lane >> 4, c = lane & 15, kq = lane >> 2, cq = lane & 3;
  const int bid = blockIdx.x, xcd = bid & 7, slot = bid >> 3;
  const int hh = xcd * 4 + (slot & 3), b = slot >> 3, half = (slot >> 2) & 1;
  const int pidx = hh * 2 + half;
  const int s8 = (slot & 3) * 2 + half;
  const int cg_start = xcd * 5 + (xcd < 3 ? xcd : 3), cg_cnt = xcd < 3 ? 6 : 5;
  const bool is_ffn = s8 < cg_cnt;
  const int cg = cg_start + (is_ffn ? s8 : 0);
  gu64* ws = (gu64*)P.ws;

  const u32 l16 = 16u * lane;
  const u32x4 xv = ld16(P.x + b * DM, l16);
  const u64 epoch = gload(ws + WS_EPOCH + 32 * b);
  u32x4 st[8];
  {
    const unsigned short* wqp = P.wq + (size_t)(32 * w) * QD + hh * DH;
    const u32 off = (u32)(8 * r * QD + 8 * c) * 2u;
#pragma unroll
    for (int i = 0; i < 8; ++i) st[i] = ld16(wqp + (size_t)i * QD, off);
  }
  const size_t kvoff = ((size_t)(b * NH + hh) * SK + (SK - WIN) + half * 1024 + 64 * w) * DH;
  const unsigned short* kp = P.kc + kvoff;
  const unsigned short* vp = P.vc + kvoff;
  u32x4 ring[8];
#pragma unroll
  for (int i = 0; i < 8; ++i) ring[i] = ldnt(kp + 512 * i, l16);
  SB();

  float xs[8];
#pragma unroll
  for (int j = 0; j < 4; ++j) { xs[2 * j] = blo(xv[j]); xs[2 * j + 1] = bhi(xv[j]); }
  {
    float s = 0.f;
#pragma unroll
    for (int j = 0; j < 8; ++j) s += xs[j];
    const float mean = wave_sum(s) * (1.f / DM);
    float q2 = 0.f;
#pragma unroll
    for (int j = 0; j < 8; ++j) { const float d = xs[j] - mean; q2 = fmaf(d, d, q2); }
    const float var = rbf(wave_sum(q2) * (1.f / DM));
    const float mb = rbf(mean), sd = rbf(sqrtf(rbf(var + EPS)));
#pragma unroll
    for (int j = 0; j < 8; ++j) xs[j] = rbf(rbf(xs[j] - mb) / sd);
  }
  {
    float qp[8];
#pragma unroll
    for (int j = 0; j < 8; ++j) qp[j] = 0.f;
#pragma unroll
    for (int i = 0; i < 8; ++i) {
      const float xn = __shfl(xs[i], 4 * w + r);
#pragma unroll
      for (int j = 0; j < 4; ++j) { qp[2 * j] = fmaf(xn, blo(st[i][j]), qp[2 * j]); qp[2 * j + 1] = fmaf(xn, bhi(st[i][j]), qp[2 * j + 1]); }
    }
    const float a0 = tr32(qp[0], qp[4]), a1 = tr32(qp[1], qp[5]), a2 = tr32(qp[2], qp[6]), a3 = tr32(qp[3], qp[7]);
    const float b0 = tr16(a0, a2), b1 = tr16(a1, a3);
    *reinterpret_cast<f32x2*>(&lds[L_PART + w * 128 + 8 * c + 4 * (r >> 1) + 2 * (r & 1)]) = f32x2{b0, b1};
  }
  const u32 woff = (u32)(kq * DM + 8 * cq) * 2u;
  {
    const size_t g0 = (size_t)(32 * w) * FF + cg * 32, g1 = g0 + (size_t)16 * FF;
    const u32 goff = (u32)(kq * FF + 8 * cq) * 2u;
    st[0] = ld16(P.wg + g0, goff); st[1] = ld16(P.wg + g1, goff);
    st[2] = ld16(P.wu + g0, goff); st[3] = ld16(P.wu + g1, goff);
    const size_t d0 = (size_t)(cg * 32) * DM + 32 * w;
    st[4] = ld16(P.wd + d0, woff); st[5] = ld16(P.wd + d0 + (size_t)16 * DM, woff);
    SB();
  }
  __syncthreads();
  if (tid < 64) {
    float s0 = 0.f, s1 = 0.f;
#pragma unroll
    for (int i = 0; i < 16; ++i) { const f32x2 v = *reinterpret_cast<const f32x2*>(&lds[L_PART + i * 128 + 2 * tid]); s0 += v[0]; s1 += v[1]; }
    const u32 lo = __float_as_uint(rbf(s0)) >> 16, hi = __float_as_uint(rbf(s1)) & 0xffff0000u;
    reinterpret_cast<u32*>(lds)[L_QBF + tid] = lo | hi;
  }
  __syncthreads();
  const u32x4 qv = *reinterpret_cast<const u32x4*>(&lds[L_QBF + 4 * c]);

  float xd[16];
#pragma unroll
  for (int i = 0; i < 16; ++i) {
    const u32x4 kv = ring[i & 7];
    float d = dot2(kv[0], qv[0], 0.f); d = dot2(kv[1], qv[1], d); d = dot2(kv[2], qv[2], d); d = dot2(kv[3], qv[3], d);
    xd[i] = d;
    const unsigned short* np = (i < 8) ? kp + 512 * (i + 8) : vp + 512 * (i - 8);
    ring[i & 7] = ldnt(np, l16);
    SB();
  }
#pragma unroll
  for (int i = 0; i < 6; ++i) *reinterpret_cast<u32x4*>(&lds[L_FFW + (i * 1024 + tid) * 4]) = st[i];
  {
    const unsigned short* wop = P.wo + (size_t)(hh * DH) * DM + 32 * w;
#pragma unroll
    for (int j = 0; j < 8; ++j) st[j] = ld16(wop + (size_t)(16 * j) * DM, woff);
    SB();
  }
#define TR_STEP(HALF, CTRL, BIT) _Pragma("unroll") for (int h_ = 0; h_ < (HALF); ++h_) { \
    const float keep_ = (c & (BIT)) ? xd[h_ + (HALF)] : xd[h_], send_ = (c & (BIT)) ? xd[h_] : xd[h_ + (HALF)]; \
    xd[h_] = keep_ + dpp<CTRL>(send_); }
  TR_STEP(8, ROR8, 8) TR_STEP(4, XOR7, 4) TR_STEP(2, XOR2, 2) TR_STEP(1, XOR1, 1)
#undef TR_STEP
  const float sc = rbf(rbf(xd[0]) * SCALE);
  const float mw = wave_max(sc);
  const float p = __builtin_amdgcn_exp2f((sc - mw) * LOG2E);
  const float lw = wave_sum(p);

  float acc[8];
#pragma unroll
  for (int j = 0; j < 8; ++j) acc[j] = 0.f;
#define V_STEP(i) { const u32x4 vv = ring[(i) & 7]; const float pi = dpp<0x150 + (i)>(p); \
    _Pragma("unroll") for (int j = 0; j < 4; ++j) { acc[2 * j] = fmaf(pi, blo(vv[j]), acc[2 * j]); acc[2 * j + 1] = fmaf(pi, bhi(vv[j]), acc[2 * j + 1]); } \
    if ((i) < 8) ring[(i) & 7] = ldnt(vp + 512 * ((i) + 8), l16); SB(); }
  V_STEP(0) V_STEP(1) V_STEP(2) V_STEP(3) V_STEP(4) V_STEP(5) V_STEP(6) V_STEP(7)
  V_STEP(8) V_STEP(9) V_STEP(10) V_STEP(11) V_STEP(12) V_STEP(13) V_STEP(14) V_STEP(15)
#undef V_STEP
  {
    const float a0 = tr32(acc[0], acc[4]), a1 = tr32(acc[1], acc[5]), a2 = tr32(acc[2], acc[6]), a3 = tr32(acc[3], acc[7]);
    const float b0 = tr16(a0, a2), b1 = tr16(a1, a3);
    *reinterpret_cast<f32x2*>(&lds[L_PART + w * 128 + 8 * c + 4 * (r >> 1) + 2 * (r & 1)]) = f32x2{b0, b1};
    if (lane == 0) *reinterpret_cast<f32x2*>(&lds[L_ML + 2 * w]) = f32x2{mw, lw};
  }
  const u64 tag = ((epoch + 1ull) & 0xffffffffffull) << 24;
  __syncthreads();
  if (tid < 128) {
    float M = -INFINITY;
#pragma unroll
    for (int i = 0; i < 16; ++i) M = fmaxf(M, lds[L_ML + 2 * i]);
    float o = 0.f, L = 0.f;
#pragma unroll
    for (int i = 0; i < 16; ++i) {
      const f32x2 ml = *reinterpret_cast<const f32x2*>(&lds[L_ML + 2 * i]);
      const float f = __builtin_amdgcn_exp2f((ml[0] - M) * LOG2E);
      o = fmaf(f, lds[L_PART + i * 128 + tid], o); L = fmaf(f, ml[1], L);
    }
    lds[L_O + (tid & 15) * 8 + (tid >> 4)] = o;
    if (tid < 2) gstore(ws + WS_ML + (size_t)(b * NP + pidx) * 2 + tid, gpack(tag, tid == 0 ? M : L));
  }
  __syncthreads();
  {
    const f32x4 o0 = *reinterpret_cast<const f32x4*>(&lds[L_O + kq * 8]), o1 = *reinterpret_cast<const f32x4*>(&lds[L_O + kq * 8 + 4]);
    float y[8];
#pragma unroll
    for (int j = 0; j < 8; ++j) y[j] = 0.f;
#pragma unroll
    for (int i = 0; i < 8; ++i) {
      const float oi = i < 4 ? o0[i & 3] : o1[i & 3];
#pragma unroll
      for (int j = 0; j < 4; ++j) { y[2 * j] = fmaf(oi, blo(st[i][j]), y[2 * j]); y[2 * j + 1] = fmaf(oi, bhi(st[i][j]), y[2 * j + 1]); }
    }
    const float a0 = tr32(y[0], y[4]), a1 = tr32(y[1], y[5]), a2 = tr32(y[2], y[6]), a3 = tr32(y[3], y[7]);
    float b0 = tr16(a0, a2), b1 = tr16(a1, a3);
    b0 += dpp<ROR8>(b0); b1 += dpp<ROR8>(b1); b0 += dpp<ROR4>(b0); b1 += dpp<ROR4>(b1);
    if ((lane & 8) == 0) {
      const int col = 32 * w + 8 * cq + 4 * (lane >> 5) + 2 * ((lane >> 4) & 1) + ((lane >> 2) & 1);
      gstore(ws + WS_Y + (size_t)(b * NP + pidx) * DM + col, gpack(tag, (lane & 4) ? b1 : b0));
    }
  }
#pragma unroll
  for (int i = 0; i < 6; ++i) st[i] = *reinterpret_cast<const u32x4*>(&lds[L_FFW + (i * 1024 + tid) * 4]);

  const int mycol = 8 * pidx + 4 * (lane >> 5) + 2 * ((lane >> 4) & 1) + ((lane >> 3) & 1);
  float hval = 0.f;
  if (w == 0) {
    gu64* yp = ws + WS_Y + (size_t)(b * NP + lane) * DM + 8 * pidx;
    gu64* mlp = ws + WS_ML + (size_t)(b * NP + lane) * 2;
    const float xres = blo((u32)P.x[b * DM + mycol]);
    u64 g[8], gm, gl;
    for (int spin = 0; spin < SPIN_CAP; ++spin) {
#pragma unroll
      for (int i = 0; i < 8; ++i) g[i] = gload(yp + i);
      gm = gload(mlp); gl = gload(mlp + 1);
      bool ok = gok(gm, tag) && gok(gl, tag);
#pragma unroll
      for (int i = 0; i < 8; ++i) ok = ok && gok(g[i], tag);
      if (__all(ok)) break;
      __builtin_amdgcn_s_sleep(1);
    }
    const float m = gval(gm), l = gval(gl), m2 = dpp<XOR1>(m), l2 = dpp<XOR1>(l);
    const float M = fmaxf(m, m2), e = __builtin_amdgcn_exp2f((m - M) * LOG2E), e2 = __builtin_amdgcn_exp2f((m2 - M) * LOG2E);
    const float f = e / fmaf(l, e, l2 * e2);
    float v[8];
#pragma unroll
    for (int i = 0; i < 8; ++i) v[i] = gval(g[i]) * f;
    const float attn = rbf(red8_64(v, lane));
    hval = rbf(xres + attn);
    if ((lane & 7) == 0) gstore(ws + WS_H + b * DM + mycol, gpack(tag, hval));
  }
  if (is_ffn) {
    if (w >= 1 && w <= 8) {
      gu64* hp = ws + WS_H + b * DM + (tid - 64);
      u64 g;
      for (int spin = 0; spin < SPIN_CAP; ++spin) {
        g = gload(hp);
        if (__all(gok(g, tag))) break;
        __builtin_amdgcn_s_sleep(1);
      }
      lds[L_H + tid - 64] = gval(g);
    }
    __syncthreads();
    {
      const f32x4 h0 = *reinterpret_cast<const f32x4*>(&lds[L_H + 8 * lane]), h1 = *reinterpret_cast<const f32x4*>(&lds[L_H + 8 * lane + 4]);
      const float ha = lds[L_H + 32 * w + kq], hb = lds[L_H + 32 * w + 16 + kq];
      const float s = ((h0[0] + h0[1]) + (h0[2] + h0[3])) + ((h1[0] + h1[1]) + (h1[2] + h1[3]));
      const float mean = wave_sum(s) * (1.f / DM);
      float q2 = 0.f;
#pragma unroll
      for (int j = 0; j < 4; ++j) { const float d0 = h0[j] - mean, d1 = h1[j] - mean; q2 = fmaf(d0, d0, q2); q2 = fmaf(d1, d1, q2); }
      const float var = rbf(wave_sum(q2) * (1.f / DM));
      const float mb = rbf(mean), sd = rbf(sqrtf(rbf(var + EPS)));
      const float na = rbf(rbf(ha - mb) / sd), nb = rbf(rbf(hb - mb) / sd);
      float gq[8], uq[8];
#pragma unroll
      for (int j = 0; j < 4; ++j) {
        gq[2 * j] = fmaf(na, blo(st[0][j]), nb * blo(st[1][j])); gq[2 * j + 1] = fmaf(na, bhi(st[0][j]), nb * bhi(st[1][j]));
        uq[2 * j] = fmaf(na, blo(st[2][j]), nb * blo(st[3][j])); uq[2 * j + 1] = fmaf(na, bhi(st[2][j]), nb * bhi(st[3][j]));
      }
      float a[8];
#pragma unroll
      for (int j = 0; j < 8; ++j) a[j] = tr32(gq[j], uq[j]);
      float bb[4];
#pragma unroll
      for (int j = 0; j < 4; ++j) { bb[j] = tr16(a[j], a[j + 4]); bb[j] += dpp<ROR8>(bb[j]); bb[j] += dpp<ROR4>(bb[j]); }
      if ((lane & 12) == 0)
        *reinterpret_cast<f32x4*>(&lds[L_RED + w * 64 + 32 * (lane >> 5) + 8 * cq + 4 * ((lane >> 4) & 1)]) = f32x4{bb[0], bb[1], bb[2], bb[3]};
    }
    __syncthreads();
    if (w == 0) {
      float s = 0.f;
#pragma unroll
      for (int i = 0; i < 16; ++i) s += lds[L_RED + i * 64 + lane];
      s = rbf(s);
      auto sw = __builtin_amdgcn_permlane32_swap(__float_as_uint(s), __float_as_uint(s), false, false);
      const float gg = __uint_as_float(sw[0]), uu = __uint_as_float(sw[1]);
      const float sg = rbf(gg / (1.f + __expf(-gg)));
      if (lane < 32) lds[L_ACT + lane] = rbf(sg * uu);
    }
    __syncthreads();
    {
      const float a0v = lds[L_ACT + kq], a1v = lds[L_ACT + 16 + kq];
      float z[8];
#pragma unroll
      for (int j = 0; j < 4; ++j) { z[2 * j] = fmaf(a0v, blo(st[4][j]), a1v * blo(st[5][j])); z[2 * j + 1] = fmaf(a0v, bhi(st[4][j]), a1v * bhi(st[5][j])); }
      const float a0 = tr32(z[0], z[4]), a1 = tr32(z[1], z[5]), a2 = tr32(z[2], z[6]), a3 = tr32(z[3], z[7]);
      float b0 = tr16(a0, a2), b1 = tr16(a1, a3);
      b0 += dpp<ROR8>(b0); b1 += dpp<ROR8>(b1); b0 += dpp<ROR4>(b0); b1 += dpp<ROR4>(b1);
      if ((lane & 8) == 0) {
        const int col = 32 * w + 8 * cq + 4 * (lane >> 5) + 2 * ((lane >> 4) & 1) + ((lane >> 2) & 1);
        gstore(ws + WS_Z + (size_t)(b * NCG + cg) * DM + col, gpack(tag, (lane & 4) ? b1 : b0));
      }
    }
  }
  if (w == 0) {
    const bool live = lane < NCG;
    gu64* zp = ws + WS_Z + (size_t)(b * NCG + (live ? lane : 0)) * DM + 8 * pidx;
    u64 g[8];
    for (int spin = 0; spin < SPIN_CAP; ++spin) {
#pragma unroll
      for (int i = 0; i < 8; ++i) g[i] = gload(zp + i);
      bool ok = true;
#pragma unroll
      for (int i = 0; i < 8; ++i) ok = ok && gok(g[i], tag);
      if (__all(ok)) break;
      __builtin_amdgcn_s_sleep(1);
    }
    float v[8];
#pragma unroll
    for (int i = 0; i < 8; ++i) v[i] = live ? gval(g[i]) : 0.f;
    const float ffn = rbf(red8_64(v, lane));
    const float o = rbf(hval + ffn);
    if ((lane & 7) == 0) P.out[b * DM + mycol] = (unsigned short)(__float_as_uint(o) >> 16);
    if (pidx == 0 && lane == 0) gstore(ws + WS_EPOCH + 32 * b, epoch + 1ull);
  }
}

extern "C" void kernel_launch(void* const* d_in, const int* in_sizes, int n_in, void* d_out, int out_size, void* d_ws, size_t ws_size,
                              hipStream_t stream) {
  Params P;
  P.x = (const unsigned short*)d_in[0]; P.kc = (const unsigned short*)d_in[1]; P.vc = (const unsigned short*)d_in[2];
  P.wq = (const unsigned short*)d_in[3]; P.wo = (const unsigned short*)d_in[4];
  P.wg = (const unsigned short*)d_in[6]; P.wu = (const unsigned short*)d_in[7]; P.wd = (const unsigned short*)d_in[8];
  P.out = (unsigned short*)d_out; P.ws = (u64*)d_ws;
  hipLaunchKernelGGL(fused_decode_block, dim3(256), dim3(NTHR), 0, stream, P);
}
```

```cpp
#include <hip/hip_runtime.h>
#include <hip/hip_bf16.h>
#include <stdint.h>

typedef unsigned u32;
typedef unsigned long long u64;
typedef u32 u32x4 __attribute__((ext_vector_type(4)));
typedef u32 u32x2 __attribute__((ext_vector_type(2)));
typedef float f32x4 __attribute__((ext_vector_type(4)));
typedef float f32x2 __attribute__((ext_vector_type(2)));
typedef short s16x4 __attribute__((ext_vector_type(4)));
typedef __bf16 bf16x2 __attribute__((ext_vector_type(2)));
typedef __bf16 bf16x8 __attribute__((ext_vector_type(8)));
#define GAS __attribute__((address_space(1)))
#define LAS __attribute__((address_space(3)))
typedef GAS u64 gu64;
#define RLX_AGENT __ATOMIC_RELAXED, __HIP_MEMORY_SCOPE_AGENT

constexpr int NB = 4, DM = 512, NH = 32, SK = 8192, DH = 128, WIN = 2048, FF = 1376, QD = NH * DH;
constexpr int NCG = FF / 32;
constexpr int NP = 64;
constexpr int NTHR = 512, NW = 8;
#define RUNAHEAD 28
#define DIST 8
#define THR 2
#define MWARM 0
#define FPOL " nt"
#define PACE 3
constexpr int RA = RUNAHEAD;
constexpr int sched(int n) { return 2 * (n - RA) < n - DIST ? 2 * (n - RA) : n - DIST; }
constexpr float SCALE = 0.08838834764831845f;
constexpr float LOG2E = 1.4426950408889634f;
constexpr float EPS = 1e-5f;
constexpr int SPIN_CAP = 1 << 18;
#define STR2(x) #x
#define STR(x) STR2(x)

constexpr size_t WS_EPOCH = 0;
constexpr size_t WS_ML = 128;
constexpr size_t WS_H = WS_ML + NB * NH * 4;
constexpr size_t WS_Q = WS_H + NB * DM;
constexpr size_t WS_O = WS_Q + (size_t)NH * NB * 4 * DH;
constexpr size_t WS_Y = WS_O + (size_t)NH * 8 * DH;
constexpr size_t WS_Z = WS_Y + (size_t)NB * 32 * NH * 16;

constexpr int L_S = 0;
constexpr int S_QP = 3072;
constexpr int L_PART = L_S + NW * 4096;
constexpr int L_QBF = L_PART + NW * 128;
constexpr int L_XN = L_QBF + 64;
constexpr int L_WMAX = L_XN + 512;
constexpr int L_WSUM = L_WMAX + 8;
constexpr int P_O = 80, P_HN = 272;
constexpr int L_ACT = L_WSUM + 8;
constexpr int L_HN = L_ACT + 4 * P_O;
constexpr int L_A2 = L_HN + 4 * P_HN;
constexpr int L_STAT = L_A2 + 64;
constexpr int L_RED = L_STAT + 64;
constexpr int L_FLAG = L_RED + NW * 256;
constexpr int L_TOTAL = L_FLAG + 4;

__device__ __forceinline__ float rbf(float x) { return (float)(__bf16)x; }
__device__ __forceinline__ float blo(u32 u) { return __builtin_bit_cast(float, u << 16); }
__device__ __forceinline__ float bhi(u32 u) { return __builtin_bit_cast(float, u & 0xffff0000u); }
__device__ __forceinline__ float dot2(u32 a, u32 b, float acc) {
  return __builtin_amdgcn_fdot2_f32_bf16(__builtin_bit_cast(bf16x2, a), __builtin_bit_cast(bf16x2, b), acc, false);
}
template <int CTRL> __device__ __forceinline__ float dpp(float x) {
  return __builtin_bit_cast(float, __builtin_amdgcn_mov_dpp(__builtin_bit_cast(int, x), CTRL, 0xf, 0xf, true));
}
constexpr int XOR1 = 0xB1, XOR2 = 0x4E, XOR7 = 0x141, ROR8 = 0x128, ROR4 = 0x124;
__device__ __forceinline__ float tr32(float a, float b) {
  auto s = __builtin_amdgcn_permlane32_swap(__float_as_uint(a), __float_as_uint(b), false, false);
  return __uint_as_float(s[0]) + __uint_as_float(s[1]);
}
__device__ __forceinline__ float tr16(float a, float b) {
  auto s = __builtin_amdgcn_permlane16_swap(__float_as_uint(a), __float_as_uint(b), false, false);
  return __uint_as_float(s[0]) + __uint_as_float(s[1]);
}
__device__ __forceinline__ float wave_sum(float v) {
  v += dpp<XOR1>(v); v += dpp<XOR2>(v); v += dpp<XOR7>(v); v += dpp<ROR8>(v);
  v = tr16(v, v); v = tr32(v, v); return v;
}
__device__ __forceinline__ float wave_max(float v) {
  v = fmaxf(v, dpp<XOR1>(v)); v = fmaxf(v, dpp<XOR2>(v)); v = fmaxf(v, dpp<XOR7>(v)); v = fmaxf(v, dpp<ROR8>(v));
  auto s = __builtin_amdgcn_permlane16_swap(__float_as_uint(v), __float_as_uint(v), false, false);
  v = fmaxf(__uint_as_float(s[0]), __uint_as_float(s[1]));
  auto t = __builtin_amdgcn_permlane32_swap(__float_as_uint(v), __float_as_uint(v), false, false);
  return fmaxf(__uint_as_float(t[0]), __uint_as_float(t[1]));
}
__device__ __forceinline__ u32 pair_pack(float v) { return (__float_as_uint(v) >> 16) | (__float_as_uint(dpp<XOR1>(v)) & 0xffff0000u); }
__device__ __forceinline__ u32x4 ldnt(const void* base, u32 off) { return __builtin_nontemporal_load(reinterpret_cast<const u32x4*>((const char*)base + off)); }
__device__ __forceinline__ u32x4 ld16(const void* base, u32 off) { return *reinterpret_cast<const u32x4*>((const char*)base + off); }
__device__ __forceinline__ void glds(const void* base, u32 off, u32 lds_dst) {
  u32 keep;
  asm volatile("s_mov_b32 %0, m0\n\ts_mov_b32 m0, %3\n\ts_nop 0\n\tglobal_load_lds_dwordx4 %1, %2 nt\n\ts_mov_b32 m0, %0"
               : "=&s"(keep) : "v"(off), "s"(base), "s"(lds_dst) : "memory");
}
__device__ __forceinline__ void glds_sc1(const void* base, u32 off, u32 lds_dst) {
  u32 keep;
  asm volatile("s_mov_b32 %0, m0\n\ts_mov_b32 m0, %3\n\ts_nop 0\n\tglobal_load_lds_dwordx4 %1, %2 sc1\n\ts_mov_b32 m0, %0"
               : "=&s"(keep) : "v"(off), "s"(base), "s"(lds_dst) : "memory");
}
__device__ __forceinline__ void glds64(u64 addr, u32 lds_dst) {
  u32 keep;
  asm volatile("s_mov_b32 %0, m0\n\ts_mov_b32 m0, %2\n\ts_nop 0\n\tglobal_load_lds_dwordx4 %1, off" FPOL "\n\ts_mov_b32 m0, %0"
               : "=&s"(keep) : "v"(addr), "s"(lds_dst) : "memory");
}
template <int PITCH> __device__ __forceinline__ u32x4 bfrag(const u32* p) {
  const u32x2 a = __builtin_bit_cast(u32x2, __builtin_amdgcn_ds_read_tr16_b64_v4i16((LAS s16x4*)p));
  const u32x2 b = __builtin_bit_cast(u32x2, __builtin_amdgcn_ds_read_tr16_b64_v4i16((LAS s16x4*)(p + 4 * PITCH)));
  return u32x4{a[0], a[1], b[0], b[1]};
}
__device__ __forceinline__ f32x4 mfma(u32x4 a, u32x4 b, f32x4 c) {
  return __builtin_amdgcn_mfma_f32_16x16x32_bf16(__builtin_bit_cast(bf16x8, a), __builtin_bit_cast(bf16x8, b), c, 0, 0, 0);
}
__device__ __forceinline__ float sel4(int g, float a, float b, float c, float d) { return g & 2 ? (g & 1 ? d : c) : (g & 1 ? b : a); }
#define SB() __builtin_amdgcn_sched_barrier(0)
#define MEMFENCE() asm volatile("" ::: "memory")
#define LDS_BARRIER() asm volatile("s_waitcnt lgkmcnt(0)\n\ts_barrier" ::: "memory")
#define LGKM0() asm volatile("s_waitcnt lgkmcnt(0)" ::: "memory")

__device__ __forceinline__ u64 gpack(u32 tag, float v) { return ((u64)tag << 32) | (u64)__float_as_uint(v); }
__device__ __forceinline__ float gval(u64 g) { return __uint_as_float((u32)g); }
__device__ __forceinline__ u32 gbad(u64 g, u32 tag) { return (u32)(g >> 32) ^ tag; }
__device__ __forceinline__ void gstore(gu64* p, u64 v) { __hip_atomic_store(p, v, RLX_AGENT); }
__device__ __forceinline__ u64 gload(gu64* p) { return __hip_atomic_load(p, RLX_AGENT); }
template <int N, int STRIDE> __device__ __forceinline__ void sweep(gu64* p, u64 (&g)[N], u32 tag) {
#pragma clang loop unroll(disable)
  for (int spin = 0; spin < SPIN_CAP; ++spin) {
#pragma unroll
    for (int i = 0; i < N; ++i) g[i] = gload(p + i * STRIDE);
    u32 bad = 0u;
#pragma unroll
    for (int i = 0; i < N; ++i) bad |= gbad(g[i], tag);
    if (__all(bad == 0u)) break;
  }
}

#define STAMP(i) do { } while (0)
struct Params {
  const unsigned short* x; const unsigned short* kc; const unsigned short* vc; const unsigned short* wq; const unsigned short* wo;
  const unsigned short* wg; const unsigned short* wu; const unsigned short* wd; unsigned short* out; u64* ws;
};

__global__ void __launch_bounds__(NTHR, 2) fused_decode_block(Params P) {
  __shared__ __attribute__((aligned(16))) u32 ldsu[L_TOTAL];
  float* lds = reinterpret_cast<float*>(ldsu);
  const int tid = threadIdx.x, lane = tid & 63, w = __builtin_amdgcn_readfirstlane(tid >> 6);
  const int r = lane >> 4, c = lane & 15;
  const int bid = blockIdx.x, xcd = bid & 7, slot = bid >> 3;
  const int hh = xcd * 4 + (slot & 3), b = slot >> 3, half = (slot >> 2) & 1;
  const int li = (slot & 3) * 4 + b, cg_cnt = xcd < 3 ? 6 : 5;
  const bool is_ffn = half == 1 && li < cg_cnt;
  const int cg = xcd * 5 + (xcd < 3 ? xcd : 3) + (is_ffn ? li : 0);
  gu64* ws = (gu64*)P.ws;
  const u32 sbase = (u32)(size_t)(LAS u32*)ldsu + (u32)w * 16384u;
  volatile LAS u32* flagp = (volatile LAS u32*)((LAS u32*)ldsu + L_FLAG);
  STAMP(0);

  const u32 l16 = 16u * lane;
  const u64 epoch = gload(ws + WS_EPOCH);
  const size_t kvoff = ((size_t)(b * NH + hh) * SK + (SK - WIN) + half * 1024 + 4 * w) * DH;
  const unsigned short* kp = P.kc + kvoff;
  const unsigned short* vp = P.vc + kvoff;
  constexpr int KST = 4096;
  u32x4 buf[64];
#define ISSUE(n) do { if ((n) < 64) { buf[(n) & 63] = ldnt(((n) < 32) ? kp + KST * (n) : vp + KST * ((n) - 32), l16); SB(); } } while (0)
#define ISSUE_AT(i) do { _Pragma("unroll") for (int n_ = RA; n_ < 64; ++n_) if (sched(n_) == (i)) { \
    if (!is_ffn) asm volatile("s_waitcnt vmcnt(" STR(THR) ")" ::: "memory"); ISSUE(n_); } } while (0)
  if (tid < 2) ldsu[L_FLAG + tid] = 0u;
  u32 tag;
  if (half == 1) {
    const u32x4 xv = ld16(P.x + (w & 3) * DM, l16);
    u32x4 wqr[4];
    {
      const unsigned short* wqp = P.wq + (size_t)(128 * b + 16 * w) * QD + hh * DH;
      const u32 off = (u32)(4 * r * QD + 8 * c) * 2u;
#pragma unroll
      for (int t = 0; t < 4; ++t) wqr[t] = ldnt(wqp + (size_t)t * QD, off);
    }
    SB();
#pragma unroll
    for (int i = 0; i < PACE; ++i) ISSUE(i);
    {
      float xs[8];
#pragma unroll
      for (int j = 0; j < 4; ++j) { xs[2 * j] = blo(xv[j]); xs[2 * j + 1] = bhi(xv[j]); }
      const float s = ((xs[0] + xs[1]) + (xs[2] + xs[3])) + ((xs[4] + xs[5]) + (xs[6] + xs[7]));
      const float mean = wave_sum(s) * (1.f / DM);
      float q2 = 0.f;
#pragma unroll
      for (int j = 0; j < 8; ++j) { const float d = xs[j] - mean; q2 = fmaf(d, d, q2); }
      const float var = rbf(wave_sum(q2) * (1.f / DM));
      const float mb = rbf(mean), inv = __builtin_amdgcn_rcpf(rbf(__builtin_amdgcn_sqrtf(rbf(var + EPS))));
#pragma unroll
      for (int j = 0; j < 8; ++j) xs[j] = rbf(rbf(xs[j] - mb) * inv);
      if (w < 4 && r == b) {
        *reinterpret_cast<f32x4*>(&lds[L_XN + w * 128 + c * 8]) = f32x4{xs[0], xs[1], xs[2], xs[3]};
        *reinterpret_cast<f32x4*>(&lds[L_XN + w * 128 + c * 8 + 4]) = f32x4{xs[4], xs[5], xs[6], xs[7]};
      }
    }
    tag = (u32)__builtin_amdgcn_readfirstlane((int)epoch) + 1u;
    LDS_BARRIER();
    STAMP(15);
    {
      float wf[4][8];
#pragma unroll
      for (int t = 0; t < 4; ++t)
#pragma unroll
        for (int j = 0; j < 4; ++j) { wf[t][2 * j] = blo(wqr[t][j]); wf[t][2 * j + 1] = bhi(wqr[t][j]); }
#pragma unroll
      for (int bb = 0; bb < 4; ++bb) {
        const f32x4 xn = *reinterpret_cast<const f32x4*>(&lds[L_XN + bb * 128 + 16 * w + 4 * r]);
        float qp[8];
#pragma unroll
        for (int j = 0; j < 8; ++j) qp[j] = fmaf(xn[0], wf[0][j], xn[1] * wf[1][j]) + fmaf(xn[2], wf[2][j], xn[3] * wf[3][j]);
        const float a0 = tr32(qp[0], qp[4]), a1 = tr32(qp[1], qp[5]), a2 = tr32(qp[2], qp[6]), a3 = tr32(qp[3], qp[7]);
        const float b0 = tr16(a0, a2), b1 = tr16(a1, a3);
        *reinterpret_cast<f32x2*>(&lds[L_S + w * 4096 + S_QP + bb * 128 + 8 * c + 4 * (r >> 1) + 2 * (r & 1)]) = f32x2{b0, b1};
      }
    }
    LDS_BARRIER();
    {
      float v[8];
#pragma unroll
      for (int i = 0; i < NW; ++i) v[i] = lds[L_S + i * 4096 + S_QP + tid];
      gstore(ws + WS_Q + ((size_t)(hh * NB + (tid >> 7)) * 4 + b) * DH + (tid & 127), gpack(tag, ((v[0] + v[1]) + (v[2] + v[3])) + ((v[4] + v[5]) + (v[6] + v[7]))));
    }
    SB();
    STAMP(14);
  } else {
    const u32 off256 = (u32)(r * DM + 8 * c) * 2u;
#pragma unroll
    for (int t = 0; t < 4; ++t) glds(P.wo + (size_t)(hh * DH + 16 * w + 4 * t) * DM + 128 * b, off256, sbase + 1024u * t);
    SB();
#pragma unroll
    for (int i = 0; i < PACE; ++i) ISSUE(i);
    tag = (u32)__builtin_amdgcn_readfirstlane((int)epoch) + 1u;
    LDS_BARRIER();
  }
  if (w < 2) {
    u64 g[4];
    sweep<4, DH>(ws + WS_Q + (size_t)(hh * NB + b) * 4 * DH + tid, g, tag);
    const u32 pk = pair_pack(rbf((gval(g[0]) + gval(g[1])) + (gval(g[2]) + gval(g[3]))));
    if ((lane & 1) == 0) ldsu[L_QBF + (tid >> 1)] = pk;
    LGKM0();
    if (lane == 0) flagp[w] = 1u;
    STAMP(17);
  }
  SB();
  {
    bool ready = w < 2;
#pragma unroll
    for (int i = PACE; i < RA; ++i) {
      if (!ready) {
        const u32x2 fl = *(volatile LAS u32x2*)flagp;
        ready = __builtin_amdgcn_readfirstlane(fl[0] & fl[1]) != 0;
        if (!ready) asm volatile("s_waitcnt vmcnt(" STR(PACE) "-1)" ::: "memory");
      }
      ISSUE(i);
    }
#pragma clang loop unroll(disable)
    for (int spin = 0; spin < (SPIN_CAP << 4); ++spin) {
      const u32x2 fl = *(volatile LAS u32x2*)flagp;
      if (__builtin_amdgcn_readfirstlane(fl[0] & fl[1]) != 0) break;
    }
    MEMFENCE();
  }
  const u32x4 qv = *reinterpret_cast<const u32x4*>(&ldsu[L_QBF + 4 * c]);
  STAMP(1);

  float sc0 = 0.f, sc1 = 0.f;
  {
    float xd[16];
#define TR_STEP(HALF, CTRL, BIT) _Pragma("unroll") for (int h_ = 0; h_ < (HALF); ++h_) { \
    const float keep_ = (c & (BIT)) ? xd[h_ + (HALF)] : xd[h_], send_ = (c & (BIT)) ? xd[h_] : xd[h_ + (HALF)]; \
    xd[h_] = keep_ + dpp<CTRL>(send_); }
#pragma unroll
    for (int i = 0; i < 32; ++i) {
      const u32x4 kv = buf[i];
      float d = dot2(kv[0], qv[0], 0.f); d = dot2(kv[1], qv[1], d); d = dot2(kv[2], qv[2], d); d = dot2(kv[3], qv[3], d);
      asm volatile("" : "+v"(d));
      xd[i & 15] = d;
      ISSUE_AT(i);
      SB();
      if ((i & 15) == 15) {
        TR_STEP(8, ROR8, 8) TR_STEP(4, XOR7, 4) TR_STEP(2, XOR2, 2) TR_STEP(1, XOR1, 1)
        const float s = rbf(rbf(xd[0]) * SCALE);
        if (i < 16) sc0 = s; else sc1 = s;
        SB();
      }
    }
#undef TR_STEP
  }
  {
    const float wm = wave_max(fmaxf(sc0, sc1));
    if (lane == 0) lds[L_WMAX + w] = wm;
  }
  LDS_BARRIER();
  float Mb;
  {
    const f32x4 m0 = *reinterpret_cast<const f32x4*>(&lds[L_WMAX]), m1 = *reinterpret_cast<const f32x4*>(&lds[L_WMAX + 4]);
    Mb = fmaxf(fmaxf(fmaxf(m0[0], m0[1]), fmaxf(m0[2], m0[3])), fmaxf(fmaxf(m1[0], m1[1]), fmaxf(m1[2], m1[3])));
  }
  const float p0 = __builtin_amdgcn_exp2f((sc0 - Mb) * LOG2E), p1 = __builtin_amdgcn_exp2f((sc1 - Mb) * LOG2E);
  {
    const float ls = wave_sum(p0 + p1);
    if (lane == 0) lds[L_WSUM + w] = ls;
  }
  const int fr_row = 8 * r + ((lane >> 2) & 3), fr_col = (lane & 3) * 2;
  u32x4 wof[4];
#pragma unroll
  for (int s = 0; s < 4; ++s) wof[s] = u32x4{0u, 0u, 0u, 0u};
  if (half == 0) {
#pragma unroll
    for (int s = 0; s < 4; ++s)
      wof[s] = bfrag<64>(ldsu + L_S + (2 * s + (fr_row >> 4)) * 4096 + (fr_row & 15) * 64 + w * 8 + fr_col);
  }
  STAMP(2);

  float acc[8];
#pragma unroll
  for (int j = 0; j < 8; ++j) acc[j] = 0.f;
  float Lb = 0.f;
  u64 gpm = 0, gpl = 0;
  gu64* pml = ws + WS_ML + (size_t)((b * NH + hh) * 2 + (half ^ 1)) * 2;
#define V_STEP(i) { const u32x4 vv = buf[32 + (i)]; const float pi = dpp<0x150 + ((i) & 15)>((i) < 16 ? p0 : p1); \
    _Pragma("unroll") for (int j = 0; j < 4; ++j) { acc[2 * j] = fmaf(pi, blo(vv[j]), acc[2 * j]); acc[2 * j + 1] = fmaf(pi, bhi(vv[j]), acc[2 * j + 1]); } \
    asm volatile("" : "+v"(acc[0]), "+v"(acc[1]), "+v"(acc[2]), "+v"(acc[3]), "+v"(acc[4]), "+v"(acc[5]), "+v"(acc[6]), "+v"(acc[7])); \
    ISSUE_AT(32 + (i)); SB(); }
  V_STEP(0) V_STEP(1) V_STEP(2) V_STEP(3)
  LDS_BARRIER();
  {
    const f32x4 s0 = *reinterpret_cast<const f32x4*>(&lds[L_WSUM]), s1 = *reinterpret_cast<const f32x4*>(&lds[L_WSUM + 4]);
    Lb = ((s0[0] + s0[1]) + (s0[2] + s0[3])) + ((s1[0] + s1[1]) + (s1[2] + s1[3]));
    if (w == 0 && lane < 2) gstore(ws + WS_ML + (size_t)((b * NH + hh) * 2 + half) * 2 + lane, gpack(tag, lane == 0 ? Mb : Lb));
  }
  SB();
  V_STEP(4) V_STEP(5) V_STEP(6) V_STEP(7) V_STEP(8) V_STEP(9) V_STEP(10) V_STEP(11) V_STEP(12) V_STEP(13) V_STEP(14) V_STEP(15)
  V_STEP(16) V_STEP(17) V_STEP(18) V_STEP(19) V_STEP(20) V_STEP(21) V_STEP(22) V_STEP(23) V_STEP(24) V_STEP(25) V_STEP(26) V_STEP(27)
  static_assert(sched(63) <= 32 + 27, "all stream loads are issued before the tail loop");
  gpm = gload(pml); gpl = gload(pml + 1);
  const int mycol = 16 * hh + c;
  SB();
  {
    {
      V_STEP(28) V_STEP(29) V_STEP(30) V_STEP(31)
      STAMP(3);
      asm volatile("" : "+v"(gpm), "+v"(gpl));
#pragma clang loop unroll(disable)
      for (int spin = 0; spin < SPIN_CAP; ++spin) {
        if ((gbad(gpm, tag) | gbad(gpl, tag)) == 0u) break;
        gpm = gload(pml); gpl = gload(pml + 1);
      }
    }
    {
      const float m2 = gval(gpm), l2 = gval(gpl);
      const float M = fmaxf(Mb, m2), e = __builtin_amdgcn_exp2f((Mb - M) * LOG2E), e2 = __builtin_amdgcn_exp2f((m2 - M) * LOG2E);
      const float f = e * __builtin_amdgcn_rcpf(fmaf(Lb, e, l2 * e2));
      const float a0 = tr32(acc[0], acc[4]), a1 = tr32(acc[1], acc[5]), a2 = tr32(acc[2], acc[6]), a3 = tr32(acc[3], acc[7]);
      const float b0 = tr16(a0, a2) * f, b1 = tr16(a1, a3) * f;
      *reinterpret_cast<f32x2*>(&lds[L_PART + w * 128 + 8 * c + 4 * (r >> 1) + 2 * (r & 1)]) = f32x2{b0, b1};
    }
    LDS_BARRIER();
    if (w < 2) {
      float v[8];
#pragma unroll
      for (int i = 0; i < NW; ++i) v[i] = lds[L_PART + i * 128 + tid];
      const u64 og = gpack(tag, ((v[0] + v[1]) + (v[2] + v[3])) + ((v[4] + v[5]) + (v[6] + v[7])));
      gstore(ws + WS_O + ((size_t)(hh * 2 + half) * NB + b) * DH + tid, og);
    }
    SB();
    STAMP(4);
    if (half == 0) {
      const u32 xraw = (u32)P.x[b * DM + mycol];
      {
        u64 g[2] = {0ull, 0ull};
        sweep<2, NB * DH>(ws + WS_O + (size_t)(hh * 2) * NB * DH + tid, g, tag);
        const u32 pk = pair_pack(rbf(gval(g[0]) + gval(g[1])));
        if ((lane & 1) == 0) ldsu[L_ACT + (tid >> 7) * P_O + ((tid & 127) >> 1)] = pk;
      }
      LDS_BARRIER();
      STAMP(13);
      {
        u32x4 af[4];
#pragma unroll
        for (int s = 0; s < 4; ++s) af[s] = *reinterpret_cast<const u32x4*>(&ldsu[L_ACT + (lane & 3) * P_O + 16 * s + 4 * r]);
        SB();
        f32x4 d = mfma(af[0], wof[0], f32x4{0.f, 0.f, 0.f, 0.f}), d2 = mfma(af[1], wof[1], f32x4{0.f, 0.f, 0.f, 0.f});
        d = mfma(af[2], wof[2], d); d2 = mfma(af[3], wof[3], d2);
        d += d2;
        const u64 yg = gpack(tag, sel4(r, d[0], d[1], d[2], d[3]));
        gstore(ws + WS_Y + (((size_t)(r * 32 + 8 * b + w)) * NH + hh) * 16 + c, yg);
      }
      STAMP(16);
      if (w == 0) {
        u64 g[11];
#pragma unroll
        for (int i = 0; i < 11; ++i) g[i] = 0ull;
        {
          u64 (&g8)[8] = reinterpret_cast<u64 (&)[8]>(g);
          sweep<8, 64>(ws + WS_Y + (size_t)(b * 32 + hh) * (NH * 16) + lane, g8, tag);
        }
        STAMP(9);
        float v = ((gval(g[0]) + gval(g[1])) + (gval(g[2]) + gval(g[3]))) + ((gval(g[4]) + gval(g[5])) + (gval(g[6]) + gval(g[7])));
        v = tr16(v, v); v = tr32(v, v);
        const float hval = rbf(blo(xraw) + rbf(v));
        if (lane < 16) gstore(ws + WS_H + b * DM + mycol, gpack(tag, hval));
        STAMP(5);
        gu64* zp = ws + WS_Z + (size_t)(b * 32 + hh) * (NCG * 16);
        const bool live = lane < NCG * 16 - 640;
        {
#pragma clang loop unroll(disable)
          for (int spin = 0; spin < SPIN_CAP; ++spin) {
#pragma unroll
            for (int i = 0; i < 10; ++i) g[i] = gload(zp + 64 * i + lane);
            g[10] = gload(zp + (live ? 640 + lane : lane));
            u32 bad = 0u;
#pragma unroll
            for (int i = 0; i < 11; ++i) bad |= gbad(g[i], tag);
            if (__all(bad == 0u)) break;
          }
        }
        STAMP(12);
        float z = (((gval(g[0]) + gval(g[1])) + (gval(g[2]) + gval(g[3]))) + ((gval(g[4]) + gval(g[5])) + (gval(g[6]) + gval(g[7])))) +
                  ((gval(g[8]) + gval(g[9])) + (live ? gval(g[10]) : 0.f));
        z = tr16(z, z); z = tr32(z, z);
        const float o = rbf(hval + rbf(z));
        {
          if (lane < 16) P.out[b * DM + mycol] = (unsigned short)(__float_as_uint(o) >> 16);
          if (bid == 0 && lane == 0) gstore(ws + WS_EPOCH, epoch + 1ull);
        }
      }
    } else if (is_ffn) {
      {
        const int rr = lane >> 3, pc = lane & 7;
        const u64 gaddr = (u64)((pc < 4 ? P.wg : P.wu) + (size_t)(64 * w + rr) * FF + cg * 32 + 8 * (pc & 3));
#pragma unroll
        for (int t = 0; t < 8; ++t) glds64(gaddr + (u64)(8 * t) * FF * 2, sbase + 1024u * t);
        const u32 off128 = (u32)(rr * DM + 8 * pc) * 2u;
#pragma unroll
        for (int t = 0; t < 4; ++t) glds(P.wd + (size_t)(cg * 32 + 8 * t) * DM + 64 * w, off128, sbase + 8192u + 1024u * t);
        asm volatile("s_waitcnt vmcnt(0)" ::: "memory");
      }
      LDS_BARRIER();
      u32x4 guf[16], wdf[4];
      const int gcol = ((lane & 2) ? 16 : 0) + (w & 3) * 4 + (lane & 1) * 2;
#pragma unroll
      for (int s = 0; s < 16; ++s) guf[s] = bfrag<32>(ldsu + L_S + (s >> 1) * 4096 + (s & 1) * 1024 + fr_row * 32 + gcol);
#pragma unroll
      for (int t = 0; t < 4; ++t) wdf[t] = bfrag<32>(ldsu + L_S + w * 4096 + 2048 + fr_row * 32 + t * 8 + fr_col);
      STAMP(18);
      if (w < 4) {
        u64 g[8];
#pragma unroll
        for (int i = 0; i < 8; ++i) g[i] = 0ull;
        sweep<8, 64>(ws + WS_H + w * DM + lane, g, tag);
        STAMP(10);
        float h[8];
#pragma unroll
        for (int i = 0; i < 8; ++i) h[i] = gval(g[i]);
        const float mean = wave_sum(((h[0] + h[1]) + (h[2] + h[3])) + ((h[4] + h[5]) + (h[6] + h[7]))) * (1.f / DM);
        float q0 = 0.f, q1 = 0.f;
#pragma unroll
        for (int i = 0; i < 4; ++i) { const float d0 = h[i] - mean, d1 = h[i + 4] - mean; q0 = fmaf(d0, d0, q0); q1 = fmaf(d1, d1, q1); }
        const float var = rbf(wave_sum(q0 + q1) * (1.f / DM));
        const float mb = rbf(mean), inv = __builtin_amdgcn_rcpf(rbf(__builtin_amdgcn_sqrtf(rbf(var + EPS))));
#pragma unroll
        for (int i = 0; i < 8; ++i) {
          const u32 pk = pair_pack(rbf(rbf(h[i] - mb) * inv));
          if ((lane & 1) == 0) ldsu[L_HN + w * P_HN + 32 * i + (lane >> 1)] = pk;
        }
      }
      LDS_BARRIER();
      STAMP(6);
      if (w < 4) {
        u32x4 af[16];
#pragma unroll
        for (int s = 0; s < 16; ++s) af[s] = *reinterpret_cast<const u32x4*>(&ldsu[L_HN + (lane & 3) * P_HN + 16 * s + 4 * r]);
        SB();
        f32x4 d0 = {0.f, 0.f, 0.f, 0.f}, d1 = {0.f, 0.f, 0.f, 0.f};
#pragma unroll
        for (int s = 0; s < 16; s += 2) { d0 = mfma(af[s], guf[s], d0); d1 = mfma(af[s + 1], guf[s + 1], d1); }
        const float v = rbf(sel4(r, d0[0] + d1[0], d0[1] + d1[1], d0[2] + d1[2], d0[3] + d1[3]));
        const float uu = dpp<ROR8>(v);
        const float sg = rbf(v * __builtin_amdgcn_rcpf(1.f + __builtin_amdgcn_exp2f(-v * LOG2E)));
        const u32 pk = pair_pack(rbf(sg * uu));
        if (c < 8 && (lane & 1) == 0) ldsu[L_A2 + r * 16 + 4 * w + (c >> 1)] = pk;
      }
      LDS_BARRIER();
      STAMP(11);
      {
        const u32x4 a = *reinterpret_cast<const u32x4*>(&ldsu[L_A2 + (lane & 3) * 16 + 4 * r]);
        f32x4 d[4];
#pragma unroll
        for (int t = 0; t < 4; ++t) d[t] = mfma(a, wdf[t], f32x4{0.f, 0.f, 0.f, 0.f});
#pragma unroll
        for (int i = 0; i < 4; ++i) {
          const u64 zg = gpack(tag, sel4(r, d[0][i], d[1][i], d[2][i], d[3][i]));
          gstore(ws + WS_Z + (((size_t)(i * 32 + 4 * w + r)) * NCG + cg) * 16 + c, zg);
        }
      }
      STAMP(7);
    }
  }
#undef V_STEP
}

extern "C" void kernel_launch(void* const* d_in, const int* in_sizes, int n_in, void* d_out, int out_size, void* d_ws, size_t ws_size,
                              hipStream_t stream) {
  Params P;
  P.x = (const unsigned short*)d_in[0]; P.kc = (const unsigned short*)d_in[1]; P.vc = (const unsigned short*)d_in[2];
  P.wq = (const unsigned short*)d_in[3]; P.wo = (const unsigned short*)d_in[4];
  P.wg = (const unsigned short*)d_in[6]; P.wu = (const unsigned short*)d_in[7]; P.wd = (const unsigned short*)d_in[8];
  P.out = (unsigned short*)d_out; P.ws = (u64*)d_ws;
  hipLaunchKernelGGL(fused_decode_block, dim3(256), dim3(NTHR), 0, stream, P);
}
```

```cpp
#include <hip/hip_runtime.h>
#include <hip/hip_bf16.h>
#include <stdint.h>

typedef unsigned u32;
typedef unsigned long long u64;
typedef u32 u32x4 __attribute__((ext_vector_type(4)));
typedef u32 u32x2 __attribute__((ext_vector_type(2)));
typedef float f32x4 __attribute__((ext_vector_type(4)));
typedef float f32x2 __attribute__((ext_vector_type(2)));
typedef short s16x4 __attribute__((ext_vector_type(4)));
typedef __bf16 bf16x2 __attribute__((ext_vector_type(2)));
typedef __bf16 bf16x8 __attribute__((ext_vector_type(8)));
#define GAS __attribute__((address_space(1)))
#define LAS __attribute__((address_space(3)))
typedef GAS u64 gu64;
#define RLX_AGENT __ATOMIC_RELAXED, __HIP_MEMORY_SCOPE_AGENT

constexpr int NB = 4, DM = 512, NH = 32, SK = 8192, DH = 128, WIN = 2048, FF = 1376, QD = NH * DH;
constexpr int NCG = FF / 32;
constexpr int NP = 64;
constexpr int NTHR = 512, NW = 8;
#define RUNAHEAD 28
#define DIST 8
#define THR 2
#define MWARM 0
#define FPOL " nt"
#define CODEPF 0
#define PACE 3
#define EARLY1 3
#define EARLY2 3
constexpr int PRE = PACE + EARLY1 + EARLY2;
constexpr int RA = RUNAHEAD;
constexpr int sched(int n) { return 2 * (n - RA) < n - DIST ? 2 * (n - RA) : n - DIST; }
constexpr float SCALE = 0.08838834764831845f;
constexpr float LOG2E = 1.4426950408889634f;
constexpr float EPS = 1e-5f;
constexpr int SPIN_CAP = 1 << 18;
#define STR2(x) #x
#define STR(x) STR2(x)

constexpr size_t WS_EPOCH = 0;
constexpr size_t WS_ML = 128;
constexpr size_t WS_H = WS_ML + NB * NH * 4;
constexpr size_t WS_Q = WS_H + NB * DM;
constexpr size_t WS_O = WS_Q + (size_t)NH * NB * 4 * DH;
constexpr size_t WS_Y = WS_O + (size_t)NH * 8 * DH;
constexpr size_t WS_Z = WS_Y + (size_t)NB * 32 * NH * 16;

constexpr int L_S = 0;
constexpr int S_QP = 3072;
constexpr int L_PART = L_S + NW * 4096;
constexpr int L_QBF = L_PART + NW * 128;
constexpr int L_XN = L_QBF + 64;
constexpr int L_WMAX = L_XN + 512;
constexpr int L_WSUM = L_WMAX + 8;
constexpr int P_O = 80, P_HN = 272;
constexpr int L_ACT = L_WSUM + 8;
constexpr int L_HN = L_ACT + 4 * P_O;
constexpr int L_A2 = L_HN + 4 * P_HN;
constexpr int L_STAT = L_A2 + 64;
constexpr int L_RED = L_STAT + 64;
constexpr int L_FLAG = L_RED + NW * 256;
constexpr int L_TOTAL = L_FLAG + 4;

__device__ __forceinline__ float rbf(float x) { return (float)(__bf16)x; }
__device__ __forceinline__ float blo(u32 u) { return __builtin_bit_cast(float, u << 16); }
__device__ __forceinline__ float bhi(u32 u) { return __builtin_bit_cast(float, u & 0xffff0000u); }
__device__ __forceinline__ float dot2(u32 a, u32 b, float acc) {
  return __builtin_amdgcn_fdot2_f32_bf16(__builtin_bit_cast(bf16x2, a), __builtin_bit_cast(bf16x2, b), acc, false);
}
template <int CTRL> __device__ __forceinline__ float dpp(float x) {
  return __builtin_bit_cast(float, __builtin_amdgcn_mov_dpp(__builtin_bit_cast(int, x), CTRL, 0xf, 0xf, true));
}
constexpr int XOR1 = 0xB1, XOR2 = 0x4E, XOR7 = 0x141, ROR8 = 0x128, ROR4 = 0x124;
__device__ __forceinline__ float tr32(float a, float b) {
  auto s = __builtin_amdgcn_permlane32_swap(__float_as_uint(a), __float_as_uint(b), false, false);
  return __uint_as_float(s[0]) + __uint_as_float(s[1]);
}
__device__ __forceinline__ float tr16(float a, float b) {
  auto s = __builtin_amdgcn_permlane16_swap(__float_as_uint(a), __float_as_uint(b), false, false);
  return __uint_as_float(s[0]) + __uint_as_float(s[1]);
}
__device__ __forceinline__ float wave_sum(float v) {
  v += dpp<XOR1>(v); v += dpp<XOR2>(v); v += dpp<XOR7>(v); v += dpp<ROR8>(v);
  v = tr16(v, v); v = tr32(v, v); return v;
}
__device__ __forceinline__ float wave_max(float v) {
  v = fmaxf(v, dpp<XOR1>(v)); v = fmaxf(v, dpp<XOR2>(v)); v = fmaxf(v, dpp<XOR7>(v)); v = fmaxf(v, dpp<ROR8>(v));
  auto s = __builtin_amdgcn_permlane16_swap(__float_as_uint(v), __float_as_uint(v), false, false);
  v = fmaxf(__uint_as_float(s[0]), __uint_as_float(s[1]));
  auto t = __builtin_amdgcn_permlane32_swap(__float_as_uint(v), __float_as_uint(v), false, false);
  return fmaxf(__uint_as_float(t[0]), __uint_as_float(t[1]));
}
__device__ __forceinline__ u32 pair_pack(float v) { return (__float_as_uint(v) >> 16) | (__float_as_uint(dpp<XOR1>(v)) & 0xffff0000u); }
__device__ __forceinline__ u32x4 ldnt(const void* base, u32 off) { return __builtin_nontemporal_load(reinterpret_cast<const u32x4*>((const char*)base + off)); }
__device__ __forceinline__ u32x4 ld16(const void* base, u32 off) { return *reinterpret_cast<const u32x4*>((const char*)base + off); }
__device__ __forceinline__ void glds(const void* base, u32 off, u32 lds_dst) {
  u32 keep;
  asm volatile("s_mov_b32 %0, m0\n\ts_mov_b32 m0, %3\n\ts_nop 0\n\tglobal_load_lds_dwordx4 %1, %2 nt\n\ts_mov_b32 m0, %0"
               : "=&s"(keep) : "v"(off), "s"(base), "s"(lds_dst) : "memory");
}
__device__ __forceinline__ void glds_sc1(const void* base, u32 off, u32 lds_dst) {
  u32 keep;
  asm volatile("s_mov_b32 %0, m0\n\ts_mov_b32 m0, %3\n\ts_nop 0\n\tglobal_load_lds_dwordx4 %1, %2 sc1\n\ts_mov_b32 m0, %0"
               : "=&s"(keep) : "v"(off), "s"(base), "s"(lds_dst) : "memory");
}
__device__ __forceinline__ void glds64(u64 addr, u32 lds_dst) {
  u32 keep;
  asm volatile("s_mov_b32 %0, m0\n\ts_mov_b32 m0, %2\n\ts_nop 0\n\tglobal_load_lds_dwordx4 %1, off" FPOL "\n\ts_mov_b32 m0, %0"
               : "=&s"(keep) : "v"(addr), "s"(lds_dst) : "memory");
}
template <int PITCH> __device__ __forceinline__ u32x4 bfrag(const u32* p) {
  const u32x2 a = __builtin_bit_cast(u32x2, __builtin_amdgcn_ds_read_tr16_b64_v4i16((LAS s16x4*)p));
  const u32x2 b = __builtin_bit_cast(u32x2, __builtin_amdgcn_ds_read_tr16_b64_v4i16((LAS s16x4*)(p + 4 * PITCH)));
  return u32x4{a[0], a[1], b[0], b[1]};
}
__device__ __forceinline__ f32x4 mfma(u32x4 a, u32x4 b, f32x4 c) {
  return __builtin_amdgcn_mfma_f32_16x16x32_bf16(__builtin_bit_cast(bf16x8, a), __builtin_bit_cast(bf16x8, b), c, 0, 0, 0);
}
__device__ __forceinline__ float sel4(int g, float a, float b, float c, float d) { return g & 2 ? (g & 1 ? d : c) : (g & 1 ? b : a); }
#define SB() __builtin_amdgcn_sched_barrier(0)
#define MEMFENCE() asm volatile("" ::: "memory")
#define LDS_BARRIER() asm volatile("s_waitcnt lgkmcnt(0)\n\ts_barrier" ::: "memory")
#define LGKM0() asm volatile("s_waitcnt lgkmcnt(0)" ::: "memory")

__device__ __forceinline__ u64 gpack(u32 tag, float v) { return ((u64)tag << 32) | (u64)__float_as_uint(v); }
__device__ __forceinline__ float gval(u64 g) { return __uint_as_float((u32)g); }
__device__ __forceinline__ u32 gbad(u64 g, u32 tag) { return (u32)(g >> 32) ^ tag; }
__device__ __forceinline__ void gstore(gu64* p, u64 v) { __hip_atomic_store(p, v, RLX_AGENT); }
__device__ __forceinline__ u64 gload(gu64* p) { return __hip_atomic_load(p, RLX_AGENT); }
template <int N, int STRIDE, int G = 1> __device__ __forceinline__ void sweep(gu64* p, u64 (&g)[N], u32 tag, gu64* plast = nullptr) {
#pragma clang loop unroll(disable)
  for (int spin = 0; spin < SPIN_CAP; ++spin) {
#pragma unroll
    for (int i = 0; i < N; ++i) g[i] = gload((plast && i == N - 1) ? plast : p + (i / G) * STRIDE + i % G);
    u32 bad = 0u;
#pragma unroll
    for (int i = 0; i < N; ++i) bad |= gbad(g[i], tag);
    if (__all(bad == 0u)) break;
  }
}
__device__ __forceinline__ u32 pk2(float lo, float hi) {
  bf16x2 v; v[0] = (__bf16)lo; v[1] = (__bf16)hi; return __builtin_bit_cast(u32, v);
}

#define STAMP(i) do { } while (0)
struct Params {
  const unsigned short* x; const unsigned short* kc; const unsigned short* vc; const unsigned short* wq; const unsigned short* wo;
  const unsigned short* wg; const unsigned short* wu; const unsigned short* wd; unsigned short* out; u64* ws;
};

__global__ void __launch_bounds__(NTHR, 2) fused_decode_block(Params P) {
  __shared__ __attribute__((aligned(16))) u32 ldsu[L_TOTAL];
  float* lds = reinterpret_cast<float*>(ldsu);
  const int tid = threadIdx.x, lane = tid & 63, w = __builtin_amdgcn_readfirstlane(tid >> 6);
  const int r = lane >> 4, c = lane & 15;
  const int bid = blockIdx.x, xcd = bid & 7, slot = bid >> 3;
  const int hh = xcd * 4 + (slot & 3), b = slot >> 3, half = (slot >> 2) & 1;
  const int li = (slot & 3) * 4 + b, cg_cnt = xcd < 3 ? 6 : 5;
  const bool is_ffn = half == 1 && li < cg_cnt;
  const int cg = xcd * 5 + (xcd < 3 ? xcd : 3) + (is_ffn ? li : 0);
  gu64* ws = (gu64*)P.ws;
  const u32 sbase = (u32)(size_t)(LAS u32*)ldsu + (u32)w * 16384u;
  volatile LAS u32* flagp = (volatile LAS u32*)((LAS u32*)ldsu + L_FLAG);
  STAMP(0);

  const u32 l16 = 16u * lane;
  const u64 epoch = gload(ws + WS_EPOCH);
  const size_t kvoff = ((size_t)(b * NH + hh) * SK + (SK - WIN) + half * 1024 + 4 * w) * DH;
  const unsigned short* kp = P.kc + kvoff;
  const unsigned short* vp = P.vc + kvoff;
  constexpr int KST = 4096;
  u32x4 buf[64];
#define ISSUE(n) do { if ((n) < 64) { buf[(n) & 63] = ldnt(((n) < 32) ? kp + KST * (n) : vp + KST * ((n) - 32), l16); SB(); } } while (0)
#define ISSUE_AT(i) do { _Pragma("unroll") for (int n_ = RA; n_ < 64; ++n_) if (sched(n_) == (i)) { \
    if (!is_ffn) asm volatile("s_waitcnt vmcnt(" STR(THR) ")" ::: "memory"); ISSUE(n_); } } while (0)
  if (tid < 2) ldsu[L_FLAG + tid] = 0u;
  u32 tag;
  if (half == 1) {
    const u32x4 xv = ld16(P.x + (w & 3) * DM, l16);
    u32x4 wqr[4];
    {
      const unsigned short* wqp = P.wq + (size_t)(128 * b + 16 * w) * QD + hh * DH;
      const u32 off = (u32)(4 * r * QD + 8 * c) * 2u;
#pragma unroll
      for (int t = 0; t < 4; ++t) wqr[t] = ldnt(wqp + (size_t)t * QD, off);
    }
    SB();
#pragma unroll
    for (int i = 0; i < PACE; ++i) ISSUE(i);
    {
      float xs[8];
#pragma unroll
      for (int j = 0; j < 4; ++j) { xs[2 * j] = blo(xv[j]); xs[2 * j + 1] = bhi(xv[j]); }
      const float s = ((xs[0] + xs[1]) + (xs[2] + xs[3])) + ((xs[4] + xs[5]) + (xs[6] + xs[7]));
      const float mean = wave_sum(s) * (1.f / DM);
      float q2 = 0.f;
#pragma unroll
      for (int j = 0; j < 8; ++j) { const float d = xs[j] - mean; q2 = fmaf(d, d, q2); }
      const float var = rbf(wave_sum(q2) * (1.f / DM));
      const float mb = rbf(mean), inv = __builtin_amdgcn_rcpf(rbf(__builtin_amdgcn_sqrtf(rbf(var + EPS))));
#pragma unroll
      for (int j = 0; j < 8; ++j) xs[j] = rbf(rbf(xs[j] - mb) * inv);
      if (w < 4 && r == b) {
        *reinterpret_cast<f32x4*>(&lds[L_XN + w * 128 + c * 8]) = f32x4{xs[0], xs[1], xs[2], xs[3]};
        *reinterpret_cast<f32x4*>(&lds[L_XN + w * 128 + c * 8 + 4]) = f32x4{xs[4], xs[5], xs[6], xs[7]};
      }
    }
    tag = (u32)__builtin_amdgcn_readfirstlane((int)epoch) + 1u;
#pragma unroll
    for (int i = PACE; i < PACE + EARLY1; ++i) ISSUE(i);
    LDS_BARRIER();
    STAMP(15);
    {
      float wf[4][8];
#pragma unroll
      for (int t = 0; t < 4; ++t)
#pragma unroll
        for (int j = 0; j < 4; ++j) { wf[t][2 * j] = blo(wqr[t][j]); wf[t][2 * j + 1] = bhi(wqr[t][j]); }
#pragma unroll
      for (int bb = 0; bb < 4; ++bb) {
        const f32x4 xn = *reinterpret_cast<const f32x4*>(&lds[L_XN + bb * 128 + 16 * w + 4 * r]);
        float qp[8];
#pragma unroll
        for (int j = 0; j < 8; ++j) qp[j] = fmaf(xn[0], wf[0][j], xn[1] * wf[1][j]) + fmaf(xn[2], wf[2][j], xn[3] * wf[3][j]);
        const float a0 = tr32(qp[0], qp[4]), a1 = tr32(qp[1], qp[5]), a2 = tr32(qp[2], qp[6]), a3 = tr32(qp[3], qp[7]);
        const float b0 = tr16(a0, a2), b1 = tr16(a1, a3);
        *reinterpret_cast<f32x2*>(&lds[L_S + w * 4096 + S_QP + bb * 128 + 8 * c + 4 * (r >> 1) + 2 * (r & 1)]) = f32x2{b0, b1};
      }
    }
    SB();
#pragma unroll
    for (int i = PACE + EARLY1; i < PRE; ++i) ISSUE(i);
    LDS_BARRIER();
    {
      float v[8];
#pragma unroll
      for (int i = 0; i < NW; ++i) v[i] = lds[L_S + i * 4096 + S_QP + tid];
      gstore(ws + WS_Q + ((size_t)(hh * NB + (tid >> 7)) * 4 + b) * DH + (tid & 127), gpack(tag, ((v[0] + v[1]) + (v[2] + v[3])) + ((v[4] + v[5]) + (v[6] + v[7]))));
    }
    SB();
    STAMP(14);
  } else {
    const u32 off256 = (u32)(r * DM + 8 * c) * 2u;
#pragma unroll
    for (int t = 0; t < 4; ++t) glds(P.wo + (size_t)(hh * DH + 16 * w + 4 * t) * DM + 128 * b, off256, sbase + 1024u * t);
    SB();
#pragma unroll
    for (int i = 0; i < PACE; ++i) ISSUE(i);
    tag = (u32)__builtin_amdgcn_readfirstlane((int)epoch) + 1u;
    LDS_BARRIER();
#pragma unroll
    for (int i = PACE; i < PRE; ++i) { asm volatile("s_waitcnt vmcnt(" STR(PACE) "-1)" ::: "memory"); ISSUE(i); }
  }
  if (w < 2) {
    u64 g[4];
    sweep<4, DH>(ws + WS_Q + (size_t)(hh * NB + b) * 4 * DH + tid, g, tag);
    const u32 pk = pair_pack(rbf((gval(g[0]) + gval(g[1])) + (gval(g[2]) + gval(g[3]))));
    if ((lane & 1) == 0) ldsu[L_QBF + (tid >> 1)] = pk;
    LGKM0();
    if (lane == 0) flagp[w] = 1u;
    STAMP(17);
  }
  SB();
  {
    bool ready = w < 2;
#pragma unroll
    for (int i = PRE; i < RA; ++i) {
      if (!ready) {
        const u32x2 fl = *(volatile LAS u32x2*)flagp;
        ready = __builtin_amdgcn_readfirstlane(fl[0] & fl[1]) != 0;
        if (!ready) asm volatile("s_waitcnt vmcnt(" STR(PACE) "-1)" ::: "memory");
      }
      ISSUE(i);
    }
#pragma clang loop unroll(disable)
    for (int spin = 0; spin < (SPIN_CAP << 4); ++spin) {
      const u32x2 fl = *(volatile LAS u32x2*)flagp;
      if (__builtin_amdgcn_readfirstlane(fl[0] & fl[1]) != 0) break;
    }
    MEMFENCE();
  }
  const u32x4 qv = *reinterpret_cast<const u32x4*>(&ldsu[L_QBF + 4 * c]);
  STAMP(1);

  float sc0 = 0.f, sc1 = 0.f;
  {
    float xd[16];
#define TR_STEP(HALF, CTRL, BIT) _Pragma("unroll") for (int h_ = 0; h_ < (HALF); ++h_) { \
    const float keep_ = (c & (BIT)) ? xd[h_ + (HALF)] : xd[h_], send_ = (c & (BIT)) ? xd[h_] : xd[h_ + (HALF)]; \
    xd[h_] = keep_ + dpp<CTRL>(send_); }
#pragma unroll
    for (int i = 0; i < 32; ++i) {
      const u32x4 kv = buf[i];
      float d = dot2(kv[0], qv[0], 0.f); d = dot2(kv[1], qv[1], d); d = dot2(kv[2], qv[2], d); d = dot2(kv[3], qv[3], d);
      asm volatile("" : "+v"(d));
      xd[i & 15] = d;
      ISSUE_AT(i);
      SB();
      if ((i & 15) == 15) {
        TR_STEP(8, ROR8, 8) TR_STEP(4, XOR7, 4) TR_STEP(2, XOR2, 2) TR_STEP(1, XOR1, 1)
        const float s = rbf(rbf(xd[0]) * SCALE);
        if (i < 16) sc0 = s; else sc1 = s;
        SB();
      }
    }
#undef TR_STEP
  }
  {
    const float wm = wave_max(fmaxf(sc0, sc1));
    if (lane == 0) lds[L_WMAX + w] = wm;
  }
  LDS_BARRIER();
  float Mb;
  {
    const f32x4 m0 = *reinterpret_cast<const f32x4*>(&lds[L_WMAX]), m1 = *reinterpret_cast<const f32x4*>(&lds[L_WMAX + 4]);
    Mb = fmaxf(fmaxf(fmaxf(m0[0], m0[1]), fmaxf(m0[2], m0[3])), fmaxf(fmaxf(m1[0], m1[1]), fmaxf(m1[2], m1[3])));
  }
  const float p0 = __builtin_amdgcn_exp2f((sc0 - Mb) * LOG2E), p1 = __builtin_amdgcn_exp2f((sc1 - Mb) * LOG2E);
  {
    const float ls = wave_sum(p0 + p1);
    if (lane == 0) lds[L_WSUM + w] = ls;
  }
  const int fr_row = 8 * r + ((lane >> 2) & 3), fr_col = (lane & 3) * 2;
  u32x4 wof[4];
#pragma unroll
  for (int s = 0; s < 4; ++s) wof[s] = u32x4{0u, 0u, 0u, 0u};
  if (half == 0) {
#pragma unroll
    for (int s = 0; s < 4; ++s)
      wof[s] = bfrag<64>(ldsu + L_S + (2 * s + (fr_row >> 4)) * 4096 + (fr_row & 15) * 64 + w * 8 + fr_col);
  }
  STAMP(2);

  float acc[8];
#pragma unroll
  for (int j = 0; j < 8; ++j) acc[j] = 0.f;
  float Lb = 0.f;
  u64 gpm = 0, gpl = 0;
  gu64* pml = ws + WS_ML + (size_t)((b * NH + hh) * 2 + (half ^ 1)) * 2;
#define V_STEP(i) { const u32x4 vv = buf[32 + (i)]; const float pi = dpp<0x150 + ((i) & 15)>((i) < 16 ? p0 : p1); \
    _Pragma("unroll") for (int j = 0; j < 4; ++j) { acc[2 * j] = fmaf(pi, blo(vv[j]), acc[2 * j]); acc[2 * j + 1] = fmaf(pi, bhi(vv[j]), acc[2 * j + 1]); } \
    asm volatile("" : "+v"(acc[0]), "+v"(acc[1]), "+v"(acc[2]), "+v"(acc[3]), "+v"(acc[4]), "+v"(acc[5]), "+v"(acc[6]), "+v"(acc[7])); \
    ISSUE_AT(32 + (i)); SB(); }
  V_STEP(0) V_STEP(1) V_STEP(2) V_STEP(3)
  LDS_BARRIER();
  {
    const f32x4 s0 = *reinterpret_cast<const f32x4*>(&lds[L_WSUM]), s1 = *reinterpret_cast<const f32x4*>(&lds[L_WSUM + 4]);
    Lb = ((s0[0] + s0[1]) + (s0[2] + s0[3])) + ((s1[0] + s1[1]) + (s1[2] + s1[3]));
    if (w == 0 && lane < 2) gstore(ws + WS_ML + (size_t)((b * NH + hh) * 2 + half) * 2 + lane, gpack(tag, lane == 0 ? Mb : Lb));
  }
  SB();
  V_STEP(4) V_STEP(5) V_STEP(6) V_STEP(7) V_STEP(8) V_STEP(9) V_STEP(10) V_STEP(11) V_STEP(12) V_STEP(13) V_STEP(14) V_STEP(15)
  gpm = gload(pml); gpl = gload(pml + 1);
  SB();
  V_STEP(16) V_STEP(17) V_STEP(18) V_STEP(19) V_STEP(20) V_STEP(21) V_STEP(22) V_STEP(23) V_STEP(24) V_STEP(25) V_STEP(26) V_STEP(27)
  static_assert(sched(63) <= 32 + 27, "all stream loads are issued before the tail loop");
  asm volatile("" : "+v"(gpm), "+v"(gpl));
#pragma clang loop unroll(disable)
  for (int spin = 0; spin < SPIN_CAP; ++spin) {
    if ((gbad(gpm, tag) | gbad(gpl, tag)) == 0u) break;
    gpm = gload(pml); gpl = gload(pml + 1);
  }
  float fsc;
  {
    const float m2 = gval(gpm), l2 = gval(gpl);
    const float M = fmaxf(Mb, m2), e = __builtin_amdgcn_exp2f((Mb - M) * LOG2E), e2 = __builtin_amdgcn_exp2f((m2 - M) * LOG2E);
    fsc = e * __builtin_amdgcn_rcpf(fmaf(Lb, e, l2 * e2));
  }
  const int mycol = 16 * hh + c;
  gu64* odst = ws + WS_O + ((size_t)(hh * 2 + half) * NB + b) * DH + tid;
  asm volatile("" : "+v"(odst), "+v"(fsc));
  SB();
  {
    V_STEP(28) V_STEP(29) V_STEP(30) V_STEP(31)
    STAMP(3);
    {
      const float f = fsc;
      const float a0 = tr32(acc[0], acc[4]), a1 = tr32(acc[1], acc[5]), a2 = tr32(acc[2], acc[6]), a3 = tr32(acc[3], acc[7]);
      const float b0 = tr16(a0, a2) * f, b1 = tr16(a1, a3) * f;
      *reinterpret_cast<f32x2*>(&lds[L_PART + w * 128 + 8 * c + 4 * (r >> 1) + 2 * (r & 1)]) = f32x2{b0, b1};
    }
    LDS_BARRIER();
    if (w < 2) {
      float v[8];
#pragma unroll
      for (int i = 0; i < NW; ++i) v[i] = lds[L_PART + i * 128 + tid];
      const u64 og = gpack(tag, ((v[0] + v[1]) + (v[2] + v[3])) + ((v[4] + v[5]) + (v[6] + v[7])));
      gstore(odst, og);
    }
    SB();
    STAMP(4);
    if (half == 0) {
      const u32 xraw = (u32)P.x[b * DM + mycol];
      gu64* ydst = ws + WS_Y + (((size_t)(r * 32 + 8 * b + w)) * NH + hh) * 16 + c;
      asm volatile("" : "+v"(ydst));
      {
        u64 g[2] = {0ull, 0ull};
        sweep<2, NB * DH>(ws + WS_O + (size_t)(hh * 2) * NB * DH + tid, g, tag);
        const u32 pk = pair_pack(rbf(gval(g[0]) + gval(g[1])));
        if ((lane & 1) == 0) ldsu[L_ACT + (tid >> 7) * P_O + ((tid & 127) >> 1)] = pk;
      }
      LDS_BARRIER();
      STAMP(13);
      {
        u32x4 af[4];
#pragma unroll
        for (int s = 0; s < 4; ++s) af[s] = *reinterpret_cast<const u32x4*>(&ldsu[L_ACT + (lane & 3) * P_O + 16 * s + 4 * r]);
        SB();
        f32x4 d = mfma(af[0], wof[0], f32x4{0.f, 0.f, 0.f, 0.f}), d2 = mfma(af[1], wof[1], f32x4{0.f, 0.f, 0.f, 0.f});
        d = mfma(af[2], wof[2], d); d2 = mfma(af[3], wof[3], d2);
        d += d2;
        const u64 yg = gpack(tag, sel4(r, d[0], d[1], d[2], d[3]));
        gstore(ydst, yg);
      }
      STAMP(16);
      if (w == 0) {
        u64 y[8];
        sweep<8, 64>(ws + WS_Y + (size_t)(b * 32 + hh) * (NH * 16) + lane, y, tag);
        STAMP(9);
        float v = ((gval(y[0]) + gval(y[1])) + (gval(y[2]) + gval(y[3]))) + ((gval(y[4]) + gval(y[5])) + (gval(y[6]) + gval(y[7])));
        v = tr16(v, v); v = tr32(v, v);
        const float hval = rbf(blo(xraw) + rbf(v));
        if (lane < 16) gstore(ws + WS_H + b * DM + mycol, gpack(tag, hval));
        STAMP(5);
        gu64* zp = ws + WS_Z + (size_t)(b * 32 + hh) * (NCG * 16);
        const bool live = lane < NCG * 16 - 640;
        u64 g[11];
        sweep<11, 64>(zp + lane, g, tag, zp + (live ? 640 + lane : lane));
        STAMP(12);
        float z = (((gval(g[0]) + gval(g[1])) + (gval(g[2]) + gval(g[3]))) + ((gval(g[4]) + gval(g[5])) + (gval(g[6]) + gval(g[7])))) +
                  ((gval(g[8]) + gval(g[9])) + (live ? gval(g[10]) : 0.f));
        z = tr16(z, z); z = tr32(z, z);
        const float o = rbf(hval + rbf(z));
        {
          if (lane < 16) P.out[b * DM + mycol] = (unsigned short)(__float_as_uint(o) >> 16);
          if (bid == 0 && lane == 0) gstore(ws + WS_EPOCH, epoch + 1ull);
        }
      }
    } else if (is_ffn) {
      {
        const int rr = lane >> 3, pc = lane & 7;
        const u64 gaddr = (u64)((pc < 4 ? P.wg : P.wu) + (size_t)(64 * w + rr) * FF + cg * 32 + 8 * (pc & 3));
#pragma unroll
        for (int t = 0; t < 8; ++t) glds64(gaddr + (u64)(8 * t) * FF * 2, sbase + 1024u * t);
        const u32 off128 = (u32)(rr * DM + 8 * pc) * 2u;
#pragma unroll
        for (int t = 0; t < 4; ++t) glds(P.wd + (size_t)(cg * 32 + 8 * t) * DM + 64 * w, off128, sbase + 8192u + 1024u * t);
        asm volatile("s_waitcnt vmcnt(0)" ::: "memory");
      }
      LDS_BARRIER();
      u32x4 guf[16], wdf[4];
      const int gcol = ((lane & 2) ? 16 : 0) + (w & 3) * 4 + (lane & 1) * 2;
#pragma unroll
      for (int s = 0; s < 16; ++s) guf[s] = bfrag<32>(ldsu + L_S + (s >> 1) * 4096 + (s & 1) * 1024 + fr_row * 32 + gcol);
#pragma unroll
      for (int t = 0; t < 4; ++t) wdf[t] = bfrag<32>(ldsu + L_S + w * 4096 + 2048 + fr_row * 32 + t * 8 + fr_col);
      gu64* zdst = ws + WS_Z + (((size_t)(4 * w + r)) * NCG + cg) * 16 + c;
      asm volatile("" : "+v"(zdst));
      STAMP(18);
      if (w < 4) {
        u64 g[8];
        sweep<8, 128, 2>(ws + WS_H + w * DM + 2 * lane, g, tag);
        STAMP(10);
        float h[8];
#pragma unroll
        for (int i = 0; i < 8; ++i) h[i] = gval(g[i]);
        const float mean = wave_sum(((h[0] + h[1]) + (h[2] + h[3])) + ((h[4] + h[5]) + (h[6] + h[7]))) * (1.f / DM);
        float q0 = 0.f, q1 = 0.f;
#pragma unroll
        for (int i = 0; i < 4; ++i) { const float d0 = h[i] - mean, d1 = h[i + 4] - mean; q0 = fmaf(d0, d0, q0); q1 = fmaf(d1, d1, q1); }
        const float mb = rbf(mean);
        u32 dk[4];
#pragma unroll
        for (int j = 0; j < 4; ++j) dk[j] = pk2(h[2 * j] - mb, h[2 * j + 1] - mb);
        const float var = rbf(wave_sum(q0 + q1) * (1.f / DM));
        const float inv = __builtin_amdgcn_rcpf(rbf(__builtin_amdgcn_sqrtf(rbf(var + EPS))));
#pragma unroll
        for (int j = 0; j < 4; ++j) ldsu[L_HN + w * P_HN + 64 * j + lane] = pk2(blo(dk[j]) * inv, bhi(dk[j]) * inv);
      }
      LDS_BARRIER();
      STAMP(6);
      if (w < 4) {
        u32x4 af[16];
#pragma unroll
        for (int s = 0; s < 16; ++s) af[s] = *reinterpret_cast<const u32x4*>(&ldsu[L_HN + (lane & 3) * P_HN + 16 * s + 4 * r]);
        SB();
        f32x4 d0 = {0.f, 0.f, 0.f, 0.f}, d1 = {0.f, 0.f, 0.f, 0.f};
#pragma unroll
        for (int s = 0; s < 16; s += 2) { d0 = mfma(af[s], guf[s], d0); d1 = mfma(af[s + 1], guf[s + 1], d1); }
        const float v = rbf(sel4(r, d0[0] + d1[0], d0[1] + d1[1], d0[2] + d1[2], d0[3] + d1[3]));
        const float uu = dpp<ROR8>(v);
        const float sg = rbf(v * __builtin_amdgcn_rcpf(1.f + __builtin_amdgcn_exp2f(-v * LOG2E)));
        const u32 pk = pair_pack(rbf(sg * uu));
        if (c < 8 && (lane & 1) == 0) ldsu[L_A2 + r * 16 + 4 * w + (c >> 1)] = pk;
      }
      LDS_BARRIER();
      STAMP(11);
      {
        const u32x4 a = *reinterpret_cast<const u32x4*>(&ldsu[L_A2 + (lane & 3) * 16 + 4 * r]);
        f32x4 d[4];
#pragma unroll
        for (int t = 0; t < 4; ++t) d[t] = mfma(a, wdf[t], f32x4{0.f, 0.f, 0.f, 0.f});
#pragma unroll
        for (int i = 0; i < 4; ++i) {
          const u64 zg = gpack(tag, sel4(r, d[0][i], d[1][i], d[2][i], d[3][i]));
          gstore(zdst + (size_t)i * (32 * NCG * 16), zg);
        }
      }
      STAMP(7);
    }
  }
#undef V_STEP
}

extern "C" void kernel_launch(void* const* d_in, const int* in_sizes, int n_in, void* d_out, int out_size, void* d_ws, size_t ws_size,
                              hipStream_t stream) {
  Params P;
  P.x = (const unsigned short*)d_in[0]; P.kc = (const unsigned short*)d_in[1]; P.vc = (const unsigned short*)d_in[2];
  P.wq = (const unsigned short*)d_in[3]; P.wo = (const unsigned short*)d_in[4];
  P.wg = (const unsigned short*)d_in[6]; P.wu = (const unsigned short*)d_in[7]; P.wd = (const unsigned short*)d_in[8];
  P.out = (unsigned short*)d_out; P.ws = (u64*)d_ws;
  hipLaunchKernelGGL(fused_decode_block, dim3(256), dim3(NTHR), 0, stream, P);
}
```

```cpp
#include <hip/hip_runtime.h>
#include <hip/hip_bf16.h>
#include <stdint.h>

typedef unsigned u32;
typedef unsigned long long u64;
typedef u32 u32x4 __attribute__((ext_vector_type(4)));
typedef u32 u32x2 __attribute__((ext_vector_type(2)));
typedef float f32x4 __attribute__((ext_vector_type(4)));
typedef float f32x2 __attribute__((ext_vector_type(2)));
typedef short s16x4 __attribute__((ext_vector_type(4)));
typedef __bf16 bf16x2 __attribute__((ext_vector_type(2)));
typedef __bf16 bf16x8 __attribute__((ext_vector_type(8)));
#define GAS __attribute__((address_space(1)))
#define LAS __attribute__((address_space(3)))
typedef GAS u64 gu64;
#define RLX_AGENT __ATOMIC_RELAXED, __HIP_MEMORY_SCOPE_AGENT

constexpr int NB = 4, DM = 512, NH = 32, SK = 8192, DH = 128, WIN = 2048, FF = 1376, QD = NH * DH;
constexpr int NCG = FF / 32;
constexpr int NP = 64;
constexpr int NTHR = 512, NW = 8;
#define RUNAHEAD 28
#define DIST 12
#define THR 2
#define MWARM 0
#define FPOL " nt"
#define CODEPF 0
#define THRN 52
#define THR2 4
#define PSLEEP 0
#define FFNEARLY 0
#define LOCKK 2
#define LOCKV 2
#define LOCKVEND 28
#define PACE 3
#define EARLY1 3
#define EARLY2 3
constexpr int PRE = PACE + EARLY1 + EARLY2;
constexpr int RA = RUNAHEAD;
constexpr int sched(int n) { return 2 * (n - RA) < n - DIST ? 2 * (n - RA) : n - DIST; }
constexpr float SCALE = 0.08838834764831845f;
constexpr float LOG2E = 1.4426950408889634f;
constexpr float EPS = 1e-5f;
constexpr int SPIN_CAP = 1 << 18;
#define STR2(x) #x
#define STR(x) STR2(x)

constexpr size_t WS_EPOCH = 0;
constexpr size_t WS_ML = 128;
constexpr size_t WS_H = WS_ML + NB * NH * 4;
constexpr size_t WS_Q = WS_H + NB * DM;
constexpr size_t WS_O = WS_Q + (size_t)NH * NB * 4 * DH;
constexpr size_t WS_Y = WS_O + (size_t)NH * 8 * DH;
constexpr size_t WS_Z = WS_Y + (size_t)NB * 32 * NH * 16;

constexpr int L_S = 0;
constexpr int S_QP = 3072;
constexpr int L_PART = L_S + NW * 4096;
constexpr int L_QBF = L_PART + NW * 128;
constexpr int L_XN = L_QBF + 64;
constexpr int L_WMAX = L_XN + 512;
constexpr int L_WSUM = L_WMAX + 8;
constexpr int P_O = 80, P_HN = 272;
constexpr int L_ACT = L_WSUM + 8;
constexpr int L_HN = L_ACT + 4 * P_O;
constexpr int L_A2 = L_HN + 4 * P_HN;
constexpr int L_STAT = L_A2 + 64;
constexpr int L_RED = L_STAT + 64;
constexpr int L_FLAG = L_RED + NW * 256;
constexpr int L_TOTAL = L_FLAG + 4;

__device__ __forceinline__ float rbf(float x) { return (float)(__bf16)x; }
__device__ __forceinline__ float blo(u32 u) { return __builtin_bit_cast(float, u << 16); }
__device__ __forceinline__ float bhi(u32 u) { return __builtin_bit_cast(float, u & 0xffff0000u); }
__device__ __forceinline__ float dot2(u32 a, u32 b, float acc) {
  return __builtin_amdgcn_fdot2_f32_bf16(__builtin_bit_cast(bf16x2, a), __builtin_bit_cast(bf16x2, b), acc, false);
}
template <int CTRL> __device__ __forceinline__ float dpp(float x) {
  return __builtin_bit_cast(float, __builtin_amdgcn_mov_dpp(__builtin_bit_cast(int, x), CTRL, 0xf, 0xf, true));
}
constexpr int XOR1 = 0xB1, XOR2 = 0x4E, XOR7 = 0x141, ROR8 = 0x128, ROR4 = 0x124;
__device__ __forceinline__ float tr32(float a, float b) {
  auto s = __builtin_amdgcn_permlane32_swap(__float_as_uint(a), __float_as_uint(b), false, false);
  return __uint_as_float(s[0]) + __uint_as_float(s[1]);
}
__device__ __forceinline__ float tr16(float a, float b) {
  auto s = __builtin_amdgcn_permlane16_swap(__float_as_uint(a), __float_as_uint(b), false, false);
  return __uint_as_float(s[0]) + __uint_as_float(s[1]);
}
__device__ __forceinline__ float wave_sum(float v) {
  v += dpp<XOR1>(v); v += dpp<XOR2>(v); v += dpp<XOR7>(v); v += dpp<ROR8>(v);
  v = tr16(v, v); v = tr32(v, v); return v;
}
__device__ __forceinline__ float wave_max(float v) {
  v = fmaxf(v, dpp<XOR1>(v)); v = fmaxf(v, dpp<XOR2>(v)); v = fmaxf(v, dpp<XOR7>(v)); v = fmaxf(v, dpp<ROR8>(v));
  auto s = __builtin_amdgcn_permlane16_swap(__float_as_uint(v), __float_as_uint(v), false, false);
  v = fmaxf(__uint_as_float(s[0]), __uint_as_float(s[1]));
  auto t = __builtin_amdgcn_permlane32_swap(__float_as_uint(v), __float_as_uint(v), false, false);
  return fmaxf(__uint_as_float(t[0]), __uint_as_float(t[1]));
}
__device__ __forceinline__ u32 pair_pack(float v) { return (__float_as_uint(v) >> 16) | (__float_as_uint(dpp<XOR1>(v)) & 0xffff0000u); }
__device__ __forceinline__ u32x4 ldnt(const void* base, u32 off) { return __builtin_nontemporal_load(reinterpret_cast<const u32x4*>((const char*)base + off)); }
__device__ __forceinline__ u32x4 ld16(const void* base, u32 off) { return *reinterpret_cast<const u32x4*>((const char*)base + off); }
__device__ __forceinline__ void glds(const void* base, u32 off, u32 lds_dst) {
  u32 keep;
  asm volatile("s_mov_b32 %0, m0\n\ts_mov_b32 m0, %3\n\ts_nop 0\n\tglobal_load_lds_dwordx4 %1, %2 nt\n\ts_mov_b32 m0, %0"
               : "=&s"(keep) : "v"(off), "s"(base), "s"(lds_dst) : "memory");
}
__device__ __forceinline__ void glds_sc1(const void* base, u32 off, u32 lds_dst) {
  u32 keep;
  asm volatile("s_mov_b32 %0, m0\n\ts_mov_b32 m0, %3\n\ts_nop 0\n\tglobal_load_lds_dwordx4 %1, %2 sc1\n\ts_mov_b32 m0, %0"
               : "=&s"(keep) : "v"(off), "s"(base), "s"(lds_dst) : "memory");
}
__device__ __forceinline__ void glds64(u64 addr, u32 lds_dst) {
  u32 keep;
  asm volatile("s_mov_b32 %0, m0\n\ts_mov_b32 m0, %2\n\ts_nop 0\n\tglobal_load_lds_dwordx4 %1, off" FPOL "\n\ts_mov_b32 m0, %0"
               : "=&s"(keep) : "v"(addr), "s"(lds_dst) : "memory");
}
template <int PITCH> __device__ __forceinline__ u32x4 bfrag(const u32* p) {
  const u32x2 a = __builtin_bit_cast(u32x2, __builtin_amdgcn_ds_read_tr16_b64_v4i16((LAS s16x4*)p));
  const u32x2 b = __builtin_bit_cast(u32x2, __builtin_amdgcn_ds_read_tr16_b64_v4i16((LAS s16x4*)(p + 4 * PITCH)));
  return u32x4{a[0], a[1], b[0], b[1]};
}
__device__ __forceinline__ f32x4 mfma(u32x4 a, u32x4 b, f32x4 c) {
  return __builtin_amdgcn_mfma_f32_16x16x32_bf16(__builtin_bit_cast(bf16x8, a), __builtin_bit_cast(bf16x8, b), c, 0, 0, 0);
}
__device__ __forceinline__ float sel4(int g, float a, float b, float c, float d) { return g & 2 ? (g & 1 ? d : c) : (g & 1 ? b : a); }
#define SB() __builtin_amdgcn_sched_barrier(0)
#define MEMFENCE() asm volatile("" ::: "memory")
#define LDS_BARRIER() asm volatile("s_waitcnt lgkmcnt(0)\n\ts_barrier" ::: "memory")
#define LGKM0() asm volatile("s_waitcnt lgkmcnt(0)" ::: "memory")

__device__ __forceinline__ u64 gpack(u32 tag, float v) { return ((u64)tag << 32) | (u64)__float_as_uint(v); }
__device__ __forceinline__ float gval(u64 g) { return __uint_as_float((u32)g); }
__device__ __forceinline__ u32 gbad(u64 g, u32 tag) { return (u32)(g >> 32) ^ tag; }
__device__ __forceinline__ void gstore(gu64* p, u64 v) { __hip_atomic_store(p, v, RLX_AGENT); }
__device__ __forceinline__ u64 gload(gu64* p) { return __hip_atomic_load(p, RLX_AGENT); }
template <int N, int STRIDE, int G = 1> __device__ __forceinline__ void sweep(gu64* p, u64 (&g)[N], u32 tag, gu64* plast = nullptr) {
#pragma clang loop unroll(disable)
  for (int spin = 0; spin < SPIN_CAP; ++spin) {
#pragma unroll
    for (int i = 0; i < N; ++i) g[i] = gload((plast && i == N - 1) ? plast : p + (i / G) * STRIDE + i % G);
    u32 bad = 0u;
#pragma unroll
    for (int i = 0; i < N; ++i) bad |= gbad(g[i], tag);
    if (__all(bad == 0u)) break;
    if (PSLEEP) __builtin_amdgcn_s_sleep(PSLEEP);
  }
}
__device__ __forceinline__ u32 pk2(float lo, float hi) {
  bf16x2 v; v[0] = (__bf16)lo; v[1] = (__bf16)hi; return __builtin_bit_cast(u32, v);
}

#define STAMP(i) do { } while (0)
struct Params {
  const unsigned short* x; const unsigned short* kc; const unsigned short* vc; const unsigned short* wq; const unsigned short* wo;
  const unsigned short* wg; const unsigned short* wu; const unsigned short* wd; unsigned short* out; u64* ws;
};

__global__ void __launch_bounds__(NTHR, 2) fused_decode_block(Params P) {
  __shared__ __attribute__((aligned(16))) u32 ldsu[L_TOTAL];
  float* lds = reinterpret_cast<float*>(ldsu);
  const int tid = threadIdx.x, lane = tid & 63, w = __builtin_amdgcn_readfirstlane(tid >> 6);
  const int r = lane >> 4, c = lane & 15;
  const int bid = blockIdx.x, xcd = bid & 7, slot = bid >> 3;
  const int hh = xcd * 4 + (slot & 3), b = slot >> 3, half = (slot >> 2) & 1;
  const int li = (slot & 3) * 4 + b, cg_cnt = xcd < 3 ? 6 : 5;
  const bool is_ffn = half == 1 && li < cg_cnt;
  const int cg = xcd * 5 + (xcd < 3 ? xcd : 3) + (is_ffn ? li : 0);
  gu64* ws = (gu64*)P.ws;
  const u32 sbase = (u32)(size_t)(LAS u32*)ldsu + (u32)w * 16384u;
  volatile LAS u32* flagp = (volatile LAS u32*)((LAS u32*)ldsu + L_FLAG);
  STAMP(0);

  const u32 l16 = 16u * lane;
  const u64 epoch = gload(ws + WS_EPOCH);
  const size_t kvoff = ((size_t)(b * NH + hh) * SK + (SK - WIN) + half * 1024 + 4 * w) * DH;
  const unsigned short* kp = P.kc + kvoff;
  const unsigned short* vp = P.vc + kvoff;
  constexpr int KST = 4096;
  u32x4 buf[64];
#define ISSUE(n) do { if ((n) < 64) { buf[(n) & 63] = ldnt(((n) < 32) ? kp + KST * (n) : vp + KST * ((n) - 32), l16); SB(); } } while (0)
#define ISSUE_AT(i) do { _Pragma("unroll") for (int n_ = RA; n_ < 64; ++n_) if (sched(n_) == (i)) { \
    if (!is_ffn) { if (n_ < THRN) asm volatile("s_waitcnt vmcnt(" STR(THR) ")" ::: "memory"); else asm volatile("s_waitcnt vmcnt(" STR(THR2) ")" ::: "memory"); } \
    ISSUE(n_); } } while (0)
#define FFN_DMA() do { \
        const int rr = lane >> 3, pc = lane & 7; \
        const u64 gaddr = (u64)((pc < 4 ? P.wg : P.wu) + (size_t)(64 * w + rr) * FF + cg * 32 + 8 * (pc & 3)); \
_Pragma("unroll") \
        for (int t = 0; t < 8; ++t) glds64(gaddr + (u64)(8 * t) * FF * 2, sbase + 1024u * t); \
        const u32 off128 = (u32)(rr * DM + 8 * pc) * 2u; \
_Pragma("unroll") \
        for (int t = 0; t < 4; ++t) glds(P.wd + (size_t)(cg * 32 + 8 * t) * DM + 64 * w, off128, sbase + 8192u + 1024u * t); \
      } while (0)
  if (tid < 2) ldsu[L_FLAG + tid] = 0u;
  u32 tag;
  if (half == 1) {
    const u32x4 xv = ld16(P.x + (w & 3) * DM, l16);
    u32x4 wqr[4];
    {
      const unsigned short* wqp = P.wq + (size_t)(128 * b + 16 * w) * QD + hh * DH;
      const u32 off = (u32)(4 * r * QD + 8 * c) * 2u;
#pragma unroll
      for (int t = 0; t < 4; ++t) wqr[t] = ldnt(wqp + (size_t)t * QD, off);
    }
    SB();
    if (FFNEARLY == 1 && is_ffn) FFN_DMA();
    SB();
#pragma unroll
    for (int i = 0; i < PACE; ++i) ISSUE(i);
    {
      float xs[8];
#pragma unroll
      for (int j = 0; j < 4; ++j) { xs[2 * j] = blo(xv[j]); xs[2 * j + 1] = bhi(xv[j]); }
      const float s = ((xs[0] + xs[1]) + (xs[2] + xs[3])) + ((xs[4] + xs[5]) + (xs[6] + xs[7]));
      const float mean = wave_sum(s) * (1.f / DM);
      float q2 = 0.f;
#pragma unroll
      for (int j = 0; j < 8; ++j) { const float d = xs[j] - mean; q2 = fmaf(d, d, q2); }
      const float var = rbf(wave_sum(q2) * (1.f / DM));
      const float mb = rbf(mean), inv = __builtin_amdgcn_rcpf(rbf(__builtin_amdgcn_sqrtf(rbf(var + EPS))));
#pragma unroll
      for (int j = 0; j < 8; ++j) xs[j] = rbf(rbf(xs[j] - mb) * inv);
      if (w < 4 && r == b) {
        *reinterpret_cast<f32x4*>(&lds[L_XN + w * 128 + c * 8]) = f32x4{xs[0], xs[1], xs[2], xs[3]};
        *reinterpret_cast<f32x4*>(&lds[L_XN + w * 128 + c * 8 + 4]) = f32x4{xs[4], xs[5], xs[6], xs[7]};
      }
    }
    tag = (u32)__builtin_amdgcn_readfirstlane((int)epoch) + 1u;
#pragma unroll
    for (int i = PACE; i < PACE + EARLY1; ++i) ISSUE(i);
    LDS_BARRIER();
    STAMP(15);
    {
      float wf[4][8];
#pragma unroll
      for (int t = 0; t < 4; ++t)
#pragma unroll
        for (int j = 0; j < 4; ++j) { wf[t][2 * j] = blo(wqr[t][j]); wf[t][2 * j + 1] = bhi(wqr[t][j]); }
#pragma unroll
      for (int bb = 0; bb < 4; ++bb) {
        const f32x4 xn = *reinterpret_cast<const f32x4*>(&lds[L_XN + bb * 128 + 16 * w + 4 * r]);
        float qp[8];
#pragma unroll
        for (int j = 0; j < 8; ++j) qp[j] = fmaf(xn[0], wf[0][j], xn[1] * wf[1][j]) + fmaf(xn[2], wf[2][j], xn[3] * wf[3][j]);
        const float a0 = tr32(qp[0], qp[4]), a1 = tr32(qp[1], qp[5]), a2 = tr32(qp[2], qp[6]), a3 = tr32(qp[3], qp[7]);
        const float b0 = tr16(a0, a2), b1 = tr16(a1, a3);
        *reinterpret_cast<f32x2*>(&lds[L_S + w * 4096 + S_QP + bb * 128 + 8 * c + 4 * (r >> 1) + 2 * (r & 1)]) = f32x2{b0, b1};
      }
    }
    SB();
#pragma unroll
    for (int i = PACE + EARLY1; i < PRE; ++i) ISSUE(i);
    LDS_BARRIER();
    {
      float v[8];
#pragma unroll
      for (int i = 0; i < NW; ++i) v[i] = lds[L_S + i * 4096 + S_QP + tid];
      gstore(ws + WS_Q + ((size_t)(hh * NB + (tid >> 7)) * 4 + b) * DH + (tid & 127), gpack(tag, ((v[0] + v[1]) + (v[2] + v[3])) + ((v[4] + v[5]) + (v[6] + v[7]))));
    }
    SB();
    STAMP(14);
    if (FFNEARLY == 2 && is_ffn) FFN_DMA();
    SB();
  } else {
    const u32 off256 = (u32)(r * DM + 8 * c) * 2u;
#pragma unroll
    for (int t = 0; t < 4; ++t) glds(P.wo + (size_t)(hh * DH + 16 * w + 4 * t) * DM + 128 * b, off256, sbase + 1024u * t);
    SB();
#pragma unroll
    for (int i = 0; i < PACE; ++i) ISSUE(i);
    tag = (u32)__builtin_amdgcn_readfirstlane((int)epoch) + 1u;
    LDS_BARRIER();
#pragma unroll
    for (int i = PACE; i < PRE; ++i) { asm volatile("s_waitcnt vmcnt(" STR(PACE) "-1)" ::: "memory"); ISSUE(i); }
  }
  if (w < 2) {
    u64 g[4];
    sweep<4, DH>(ws + WS_Q + (size_t)(hh * NB + b) * 4 * DH + tid, g, tag);
    const u32 pk = pair_pack(rbf((gval(g[0]) + gval(g[1])) + (gval(g[2]) + gval(g[3]))));
    if ((lane & 1) == 0) ldsu[L_QBF + (tid >> 1)] = pk;
    LGKM0();
    if (lane == 0) flagp[w] = 1u;
    STAMP(17);
  }
  SB();
  {
    bool ready = w < 2;
#pragma unroll
    for (int i = PRE; i < RA; ++i) {
      if (!ready) {
        const u32x2 fl = *(volatile LAS u32x2*)flagp;
        ready = __builtin_amdgcn_readfirstlane(fl[0] & fl[1]) != 0;
        if (!ready) asm volatile("s_waitcnt vmcnt(" STR(PACE) "-1)" ::: "memory");
      }
      ISSUE(i);
    }
#pragma clang loop unroll(disable)
    for (int spin = 0; spin < (SPIN_CAP << 4); ++spin) {
      const u32x2 fl = *(volatile LAS u32x2*)flagp;
      if (__builtin_amdgcn_readfirstlane(fl[0] & fl[1]) != 0) break;
    }
    MEMFENCE();
  }
  const u32x4 qv = *reinterpret_cast<const u32x4*>(&ldsu[L_QBF + 4 * c]);
  STAMP(1);

  float sc0 = 0.f, sc1 = 0.f;
  {
    float xd[16];
#define TR_STEP(HALF, CTRL, BIT) _Pragma("unroll") for (int h_ = 0; h_ < (HALF); ++h_) { \
    const float keep_ = (c & (BIT)) ? xd[h_ + (HALF)] : xd[h_], send_ = (c & (BIT)) ? xd[h_] : xd[h_ + (HALF)]; \
    xd[h_] = keep_ + dpp<CTRL>(send_); }
#pragma unroll
    for (int i = 0; i < 32; ++i) {
      const u32x4 kv = buf[i];
      float d = dot2(kv[0], qv[0], 0.f); d = dot2(kv[1], qv[1], d); d = dot2(kv[2], qv[2], d); d = dot2(kv[3], qv[3], d);
      asm volatile("" : "+v"(d));
      xd[i & 15] = d;
      ISSUE_AT(i);
      SB();
      if (LOCKK && i < 31 && (i % LOCKK) == LOCKK - 1) __builtin_amdgcn_s_barrier();
      if ((i & 15) == 15) {
        TR_STEP(8, ROR8, 8) TR_STEP(4, XOR7, 4) TR_STEP(2, XOR2, 2) TR_STEP(1, XOR1, 1)
        const float s = rbf(rbf(xd[0]) * SCALE);
        if (i < 16) sc0 = s; else sc1 = s;
        SB();
      }
    }
#undef TR_STEP
  }
  {
    const float wm = wave_max(fmaxf(sc0, sc1));
    if (lane == 0) lds[L_WMAX + w] = wm;
  }
  LDS_BARRIER();
  float Mb;
  {
    const f32x4 m0 = *reinterpret_cast<const f32x4*>(&lds[L_WMAX]), m1 = *reinterpret_cast<const f32x4*>(&lds[L_WMAX + 4]);
    Mb = fmaxf(fmaxf(fmaxf(m0[0], m0[1]), fmaxf(m0[2], m0[3])), fmaxf(fmaxf(m1[0], m1[1]), fmaxf(m1[2], m1[3])));
  }
  const float p0 = __builtin_amdgcn_exp2f((sc0 - Mb) * LOG2E), p1 = __builtin_amdgcn_exp2f((sc1 - Mb) * LOG2E);
  {
    const float ls = wave_sum(p0 + p1);
    if (lane == 0) lds[L_WSUM + w] = ls;
  }
  const int fr_row = 8 * r + ((lane >> 2) & 3), fr_col = (lane & 3) * 2;
  u32x4 wof[4];
#pragma unroll
  for (int s = 0; s < 4; ++s) wof[s] = u32x4{0u, 0u, 0u, 0u};
  if (half == 0) {
#pragma unroll
    for (int s = 0; s < 4; ++s)
      wof[s] = bfrag<64>(ldsu + L_S + (2 * s + (fr_row >> 4)) * 4096 + (fr_row & 15) * 64 + w * 8 + fr_col);
  }
  STAMP(2);

  float acc[8];
#pragma unroll
  for (int j = 0; j < 8; ++j) acc[j] = 0.f;
  float Lb = 0.f;
  u64 gpm = 0, gpl = 0;
  gu64* pml = ws + WS_ML + (size_t)((b * NH + hh) * 2 + (half ^ 1)) * 2;
#define V_STEP(i) { const u32x4 vv = buf[32 + (i)]; const float pi = dpp<0x150 + ((i) & 15)>((i) < 16 ? p0 : p1); \
    _Pragma("unroll") for (int j = 0; j < 4; ++j) { acc[2 * j] = fmaf(pi, blo(vv[j]), acc[2 * j]); acc[2 * j + 1] = fmaf(pi, bhi(vv[j]), acc[2 * j + 1]); } \
    asm volatile("" : "+v"(acc[0]), "+v"(acc[1]), "+v"(acc[2]), "+v"(acc[3]), "+v"(acc[4]), "+v"(acc[5]), "+v"(acc[6]), "+v"(acc[7])); \
    ISSUE_AT(32 + (i)); SB(); if (LOCKV && (i) > 3 && (i) < LOCKVEND && ((i) % LOCKV) == LOCKV - 1) __builtin_amdgcn_s_barrier(); }
  V_STEP(0) V_STEP(1) V_STEP(2) V_STEP(3)
  LDS_BARRIER();
  {
    const f32x4 s0 = *reinterpret_cast<const f32x4*>(&lds[L_WSUM]), s1 = *reinterpret_cast<const f32x4*>(&lds[L_WSUM + 4]);
    Lb = ((s0[0] + s0[1]) + (s0[2] + s0[3])) + ((s1[0] + s1[1]) + (s1[2] + s1[3]));
    if (w == 0 && lane < 2) gstore(ws + WS_ML + (size_t)((b * NH + hh) * 2 + half) * 2 + lane, gpack(tag, lane == 0 ? Mb : Lb));
  }
  SB();
  V_STEP(4) V_STEP(5) V_STEP(6) V_STEP(7) V_STEP(8) V_STEP(9) V_STEP(10) V_STEP(11) V_STEP(12) V_STEP(13) V_STEP(14) V_STEP(15)
  gpm = gload(pml); gpl = gload(pml + 1);
  SB();
  V_STEP(16) V_STEP(17) V_STEP(18) V_STEP(19) V_STEP(20) V_STEP(21) V_STEP(22) V_STEP(23) V_STEP(24) V_STEP(25) V_STEP(26) V_STEP(27)
  static_assert(sched(63) <= 32 + 27, "all stream loads are issued before the tail loop");
  asm volatile("" : "+v"(gpm), "+v"(gpl));
#pragma clang loop unroll(disable)
  for (int spin = 0; spin < SPIN_CAP; ++spin) {
    if ((gbad(gpm, tag) | gbad(gpl, tag)) == 0u) break;
    gpm = gload(pml); gpl = gload(pml + 1);
  }
  float fsc;
  {
    const float m2 = gval(gpm), l2 = gval(gpl);
    const float M = fmaxf(Mb, m2), e = __builtin_amdgcn_exp2f((Mb - M) * LOG2E), e2 = __builtin_amdgcn_exp2f((m2 - M) * LOG2E);
    fsc = e * __builtin_amdgcn_rcpf(fmaf(Lb, e, l2 * e2));
  }
  const int mycol = 16 * hh + c;
  gu64* odst = ws + WS_O + ((size_t)(hh * 2 + half) * NB + b) * DH + tid;
  asm volatile("" : "+v"(odst), "+v"(fsc));
  SB();
  {
    V_STEP(28) V_STEP(29) V_STEP(30) V_STEP(31)
    STAMP(3);
    {
      const float f = fsc;
      const float a0 = tr32(acc[0], acc[4]), a1 = tr32(acc[1], acc[5]), a2 = tr32(acc[2], acc[6]), a3 = tr32(acc[3], acc[7]);
      const float b0 = tr16(a0, a2) * f, b1 = tr16(a1, a3) * f;
      *reinterpret_cast<f32x2*>(&lds[L_PART + w * 128 + 8 * c + 4 * (r >> 1) + 2 * (r & 1)]) = f32x2{b0, b1};
    }
    LDS_BARRIER();
    if (w < 2) {
      float v[8];
#pragma unroll
      for (int i = 0; i < NW; ++i) v[i] = lds[L_PART + i * 128 + tid];
      const u64 og = gpack(tag, ((v[0] + v[1]) + (v[2] + v[3])) + ((v[4] + v[5]) + (v[6] + v[7])));
      gstore(odst, og);
    }
    SB();
    STAMP(4);
    if (half == 0) {
      const u32 xraw = (u32)P.x[b * DM + mycol];
      gu64* ydst = ws + WS_Y + (((size_t)(r * 32 + 8 * b + w)) * NH + hh) * 16 + c;
      asm volatile("" : "+v"(ydst));
      {
        u64 g[2] = {0ull, 0ull};
        sweep<2, NB * DH>(ws + WS_O + (size_t)(hh * 2) * NB * DH + tid, g, tag);
        const u32 pk = pair_pack(rbf(gval(g[0]) + gval(g[1])));
        if ((lane & 1) == 0) ldsu[L_ACT + (tid >> 7) * P_O + ((tid & 127) >> 1)] = pk;
      }
      LDS_BARRIER();
      STAMP(13);
      {
        u32x4 af[4];
#pragma unroll
        for (int s = 0; s < 4; ++s) af[s] = *reinterpret_cast<const u32x4*>(&ldsu[L_ACT + (lane & 3) * P_O + 16 * s + 4 * r]);
        SB();
        f32x4 d = mfma(af[0], wof[0], f32x4{0.f, 0.f, 0.f, 0.f}), d2 = mfma(af[1], wof[1], f32x4{0.f, 0.f, 0.f, 0.f});
        d = mfma(af[2], wof[2], d); d2 = mfma(af[3], wof[3], d2);
        d += d2;
        const u64 yg = gpack(tag, sel4(r, d[0], d[1], d[2], d[3]));
        gstore(ydst, yg);
      }
      STAMP(16);
      if (w == 0) {
        u64 y[8];
        sweep<8, 64>(ws + WS_Y + (size_t)(b * 32 + hh) * (NH * 16) + lane, y, tag);
        STAMP(9);
        float v = ((gval(y[0]) + gval(y[1])) + (gval(y[2]) + gval(y[3]))) + ((gval(y[4]) + gval(y[5])) + (gval(y[6]) + gval(y[7])));
        v = tr16(v, v); v = tr32(v, v);
        const float hval = rbf(blo(xraw) + rbf(v));
        if (lane < 16) gstore(ws + WS_H + b * DM + mycol, gpack(tag, hval));
        STAMP(5);
        gu64* zp = ws + WS_Z + (size_t)(b * 32 + hh) * (NCG * 16);
        const bool live = lane < NCG * 16 - 640;
        u64 g[11];
        sweep<11, 64>(zp + lane, g, tag, zp + (live ? 640 + lane : lane));
        STAMP(12);
        float z = (((gval(g[0]) + gval(g[1])) + (gval(g[2]) + gval(g[3]))) + ((gval(g[4]) + gval(g[5])) + (gval(g[6]) + gval(g[7])))) +
                  ((gval(g[8]) + gval(g[9])) + (live ? gval(g[10]) : 0.f));
        z = tr16(z, z); z = tr32(z, z);
        const float o = rbf(hval + rbf(z));
        {
          if (lane < 16) P.out[b * DM + mycol] = (unsigned short)(__float_as_uint(o) >> 16);
          if (bid == 0 && lane == 0) gstore(ws + WS_EPOCH, epoch + 1ull);
        }
      }
    } else if (is_ffn) {
      if (FFNEARLY == 0) { FFN_DMA(); asm volatile("s_waitcnt vmcnt(0)" ::: "memory"); }
      LDS_BARRIER();
      u32x4 guf[16], wdf[4];
      const int gcol = ((lane & 2) ? 16 : 0) + (w & 3) * 4 + (lane & 1) * 2;
#pragma unroll
      for (int s = 0; s < 16; ++s) guf[s] = bfrag<32>(ldsu + L_S + (s >> 1) * 4096 + (s & 1) * 1024 + fr_row * 32 + gcol);
#pragma unroll
      for (int t = 0; t < 4; ++t) wdf[t] = bfrag<32>(ldsu + L_S + w * 4096 + 2048 + fr_row * 32 + t * 8 + fr_col);
      gu64* zdst = ws + WS_Z + (((size_t)(4 * w + r)) * NCG + cg) * 16 + c;
      asm volatile("" : "+v"(zdst));
      STAMP(18);
      if (w < 4) {
        u64 g[8];
        sweep<8, 128, 2>(ws + WS_H + w * DM + 2 * lane, g, tag);
        STAMP(10);
        float h[8];
#pragma unroll
        for (int i = 0; i < 8; ++i) h[i] = gval(g[i]);
        const float mean = wave_sum(((h[0] + h[1]) + (h[2] + h[3])) + ((h[4] + h[5]) + (h[6] + h[7]))) * (1.f / DM);
        float q0 = 0.f, q1 = 0.f;
#pragma unroll
        for (int i = 0; i < 4; ++i) { const float d0 = h[i] - mean, d1 = h[i + 4] - mean; q0 = fmaf(d0, d0, q0); q1 = fmaf(d1, d1, q1); }
        const float mb = rbf(mean);
        u32 dk[4];
#pragma unroll
        for (int j = 0; j < 4; ++j) dk[j] = pk2(h[2 * j] - mb, h[2 * j + 1] - mb);
        const float var = rbf(wave_sum(q0 + q1) * (1.f / DM));
        const float inv = __builtin_amdgcn_rcpf(rbf(__builtin_amdgcn_sqrtf(rbf(var + EPS))));
#pragma unroll
        for (int j = 0; j < 4; ++j) ldsu[L_HN + w * P_HN + 64 * j + lane] = pk2(blo(dk[j]) * inv, bhi(dk[j]) * inv);
      }
      LDS_BARRIER();
      STAMP(6);
      if (w < 4) {
        u32x4 af[16];
#pragma unroll
        for (int s = 0; s < 16; ++s) af[s] = *reinterpret_cast<const u32x4*>(&ldsu[L_HN + (lane & 3) * P_HN + 16 * s + 4 * r]);
        SB();
        f32x4 d0 = {0.f, 0.f, 0.f, 0.f}, d1 = {0.f, 0.f, 0.f, 0.f};
#pragma unroll
        for (int s = 0; s < 16; s += 2) { d0 = mfma(af[s], guf[s], d0); d1 = mfma(af[s + 1], guf[s + 1], d1); }
        const float v = rbf(sel4(r, d0[0] + d1[0], d0[1] + d1[1], d0[2] + d1[2], d0[3] + d1[3]));
        const float uu = dpp<ROR8>(v);
        const float sg = rbf(v * __builtin_amdgcn_rcpf(1.f + __builtin_amdgcn_exp2f(-v * LOG2E)));
        const u32 pk = pair_pack(rbf(sg * uu));
        if (c < 8 && (lane & 1) == 0) ldsu[L_A2 + r * 16 + 4 * w + (c >> 1)] = pk;
      }
      LDS_BARRIER();
      STAMP(11);
      {
        const u32x4 a = *reinterpret_cast<const u32x4*>(&ldsu[L_A2 + (lane & 3) * 16 + 4 * r]);
        f32x4 d[4];
#pragma unroll
        for (int t = 0; t < 4; ++t) d[t] = mfma(a, wdf[t], f32x4{0.f, 0.f, 0.f, 0.f});
#pragma unroll
        for (int i = 0; i < 4; ++i) {
          const u64 zg = gpack(tag, sel4(r, d[0][i], d[1][i], d[2][i], d[3][i]));
          gstore(zdst + (size_t)i * (32 * NCG * 16), zg);
        }
      }
      STAMP(7);
    }
  }
#undef V_STEP
}

extern "C" void kernel_launch(void* const* d_in, const int* in_sizes, int n_in, void* d_out, int out_size, void* d_ws, size_t ws_size,
                              hipStream_t stream) {
  Params P;
  P.x = (const unsigned short*)d_in[0]; P.kc = (const unsigned short*)d_in[1]; P.vc = (const unsigned short*)d_in[2];
  P.wq = (const unsigned short*)d_in[3]; P.wo = (const unsigned short*)d_in[4];
  P.wg = (const unsigned short*)d_in[6]; P.wu = (const unsigned short*)d_in[7]; P.wd = (const unsigned short*)d_in[8];
  P.out = (unsigned short*)d_out; P.ws = (u64*)d_ws;
  hipLaunchKernelGGL(fused_decode_block, dim3(256), dim3(NTHR), 0, stream, P);
}
```

```cpp
#include <hip/hip_runtime.h>
#include <hip/hip_bf16.h>
#include <stdint.h>

typedef unsigned u32;
typedef unsigned long long u64;
typedef u32 u32x4 __attribute__((ext_vector_type(4)));
typedef u32 u32x2 __attribute__((ext_vector_type(2)));
typedef float f32x4 __attribute__((ext_vector_type(4)));
typedef float f32x2 __attribute__((ext_vector_type(2)));
typedef short s16x4 __attribute__((ext_vector_type(4)));
typedef __bf16 bf16x2 __attribute__((ext_vector_type(2)));
typedef __bf16 bf16x8 __attribute__((ext_vector_type(8)));
#define GAS __attribute__((address_space(1)))
#define LAS __attribute__((address_space(3)))
typedef GAS u64 gu64;
#define RLX_AGENT __ATOMIC_RELAXED, __HIP_MEMORY_SCOPE_AGENT

constexpr int NB = 4, DM = 512, NH = 32, SK = 8192, DH = 128, WIN = 2048, FF = 1376, QD = NH * DH;
constexpr int NCG = FF / 32;
constexpr int NP = 64;
constexpr int NTHR = 512, NW = 8;
#define RUNAHEAD 28
#define DIST 12
#define THR 2
#define MWARM 0
#define FPOL " nt"
#define CODEPF 0
#define THRN 52
#define THR2 5
#define PSLEEP 0
#define FFNEARLY 0
#define LOCKK 2
#define LOCKV 2
#define LOCKVEND 28
#define PACED 1
#define TIEND 2000
#define TISTEP 30
#define TIMARGIN 30
#define PACE 3
#define EARLY1 3
#define EARLY2 3
constexpr int PRE = PACE + EARLY1 + EARLY2;
constexpr int RA = RUNAHEAD;
constexpr int sched(int n) { return 2 * (n - RA) < n - DIST ? 2 * (n - RA) : n - DIST; }
constexpr float SCALE = 0.08838834764831845f;
constexpr float LOG2E = 1.4426950408889634f;
constexpr float EPS = 1e-5f;
constexpr int SPIN_CAP = 1 << 18;
#define STR2(x) #x
#define STR(x) STR2(x)

constexpr size_t WS_EPOCH = 0;
constexpr size_t WS_ML = 128;
constexpr size_t WS_H = WS_ML + NB * NH * 4;
constexpr size_t WS_Q = WS_H + NB * DM;
constexpr size_t WS_O = WS_Q + (size_t)NH * NB * 4 * DH;
constexpr size_t WS_Y = WS_O + (size_t)NH * 8 * DH;
constexpr size_t WS_Z = WS_Y + (size_t)NB * 32 * NH * 16;

constexpr int L_S = 0;
constexpr int S_QP = 3072;
constexpr int L_PART = L_S + NW * 4096;
constexpr int L_QBF = L_PART + NW * 128;
constexpr int L_XN = L_QBF + 64;
constexpr int L_WMAX = L_XN + 512;
constexpr int L_WSUM = L_WMAX + 8;
constexpr int P_O = 80, P_HN = 272;
constexpr int L_ACT = L_WSUM + 8;
constexpr int L_HN = L_ACT + 4 * P_O;
constexpr int L_A2 = L_HN + 4 * P_HN;
constexpr int L_STAT = L_A2 + 64;
constexpr int L_RED = L_STAT + 64;
constexpr int L_FLAG = L_RED + NW * 256;
constexpr int L_TOTAL = L_FLAG + 4;

__device__ __forceinline__ float rbf(float x) { return (float)(__bf16)x; }
__device__ __forceinline__ float blo(u32 u) { return __builtin_bit_cast(float, u << 16); }
__device__ __forceinline__ float bhi(u32 u) { return __builtin_bit_cast(float, u & 0xffff0000u); }
__device__ __forceinline__ float dot2(u32 a, u32 b, float acc) {
  return __builtin_amdgcn_fdot2_f32_bf16(__builtin_bit_cast(bf16x2, a), __builtin_bit_cast(bf16x2, b), acc, false);
}
template <int CTRL> __device__ __forceinline__ float dpp(float x) {
  return __builtin_bit_cast(float, __builtin_amdgcn_mov_dpp(__builtin_bit_cast(int, x), CTRL, 0xf, 0xf, true));
}
constexpr int XOR1 = 0xB1, XOR2 = 0x4E, XOR7 = 0x141, ROR8 = 0x128, ROR4 = 0x124;
__device__ __forceinline__ float tr32(float a, float b) {
  auto s = __builtin_amdgcn_permlane32_swap(__float_as_uint(a), __float_as_uint(b), false, false);
  return __uint_as_float(s[0]) + __uint_as_float(s[1]);
}
__device__ __forceinline__ float tr16(float a, float b) {
  auto s = __builtin_amdgcn_permlane16_swap(__float_as_uint(a), __float_as_uint(b), false, false);
  return __uint_as_float(s[0]) + __uint_as_float(s[1]);
}
__device__ __forceinline__ float wave_sum(float v) {
  v += dpp<XOR1>(v); v += dpp<XOR2>(v); v += dpp<XOR7>(v); v += dpp<ROR8>(v);
  v = tr16(v, v); v = tr32(v, v); return v;
}
__device__ __forceinline__ float wave_max(float v) {
  v = fmaxf(v, dpp<XOR1>(v)); v = fmaxf(v, dpp<XOR2>(v)); v = fmaxf(v, dpp<XOR7>(v)); v = fmaxf(v, dpp<ROR8>(v));
  auto s = __builtin_amdgcn_permlane16_swap(__float_as_uint(v), __float_as_uint(v), false, false);
  v = fmaxf(__uint_as_float(s[0]), __uint_as_float(s[1]));
  auto t = __builtin_amdgcn_permlane32_swap(__float_as_uint(v), __float_as_uint(v), false, false);
  return fmaxf(__uint_as_float(t[0]), __uint_as_float(t[1]));
}
__device__ __forceinline__ u32 pair_pack(float v) { return (__float_as_uint(v) >> 16) | (__float_as_uint(dpp<XOR1>(v)) & 0xffff0000u); }
__device__ __forceinline__ u32x4 ldnt(const void* base, u32 off) { return __builtin_nontemporal_load(reinterpret_cast<const u32x4*>((const char*)base + off)); }
__device__ __forceinline__ u32x4 ld16(const void* base, u32 off) { return *reinterpret_cast<const u32x4*>((const char*)base + off); }
__device__ __forceinline__ void glds(const void* base, u32 off, u32 lds_dst) {
  u32 keep;
  asm volatile("s_mov_b32 %0, m0\n\ts_mov_b32 m0, %3\n\ts_nop 0\n\tglobal_load_lds_dwordx4 %1, %2 nt\n\ts_mov_b32 m0, %0"
               : "=&s"(keep) : "v"(off), "s"(base), "s"(lds_dst) : "memory");
}
__device__ __forceinline__ void glds_sc1(const void* base, u32 off, u32 lds_dst) {
  u32 keep;
  asm volatile("s_mov_b32 %0, m0\n\ts_mov_b32 m0, %3\n\ts_nop 0\n\tglobal_load_lds_dwordx4 %1, %2 sc1\n\ts_mov_b32 m0, %0"
               : "=&s"(keep) : "v"(off), "s"(base), "s"(lds_dst) : "memory");
}
__device__ __forceinline__ void glds64(u64 addr, u32 lds_dst) {
  u32 keep;
  asm volatile("s_mov_b32 %0, m0\n\ts_mov_b32 m0, %2\n\ts_nop 0\n\tglobal_load_lds_dwordx4 %1, off" FPOL "\n\ts_mov_b32 m0, %0"
               : "=&s"(keep) : "v"(addr), "s"(lds_dst) : "memory");
}
template <int PITCH> __device__ __forceinline__ u32x4 bfrag(const u32* p) {
  const u32x2 a = __builtin_bit_cast(u32x2, __builtin_amdgcn_ds_read_tr16_b64_v4i16((LAS s16x4*)p));
  const u32x2 b = __builtin_bit_cast(u32x2, __builtin_amdgcn_ds_read_tr16_b64_v4i16((LAS s16x4*)(p + 4 * PITCH)));
  return u32x4{a[0], a[1], b[0], b[1]};
}
__device__ __forceinline__ f32x4 mfma(u32x4 a, u32x4 b, f32x4 c) {
  return __builtin_amdgcn_mfma_f32_16x16x32_bf16(__builtin_bit_cast(bf16x8, a), __builtin_bit_cast(bf16x8, b), c, 0, 0, 0);
}
__device__ __forceinline__ float sel4(int g, float a, float b, float c, float d) { return g & 2 ? (g & 1 ? d : c) : (g & 1 ? b : a); }
#define SB() __builtin_amdgcn_sched_barrier(0)
#define MEMFENCE() asm volatile("" ::: "memory")
#define LDS_BARRIER() asm volatile("s_waitcnt lgkmcnt(0)\n\ts_barrier" ::: "memory")
#define LGKM0() asm volatile("s_waitcnt lgkmcnt(0)" ::: "memory")

__device__ __forceinline__ u64 gpack(u32 tag, float v) { return ((u64)tag << 32) | (u64)__float_as_uint(v); }
__device__ __forceinline__ float gval(u64 g) { return __uint_as_float((u32)g); }
__device__ __forceinline__ u32 gbad(u64 g, u32 tag) { return (u32)(g >> 32) ^ tag; }
__device__ __forceinline__ void gstore(gu64* p, u64 v) { __hip_atomic_store(p, v, RLX_AGENT); }
__device__ __forceinline__ u64 gload(gu64* p) { return __hip_atomic_load(p, RLX_AGENT); }
template <int N, int STRIDE, int G = 1> __device__ __forceinline__ void sweep(gu64* p, u64 (&g)[N], u32 tag, gu64* plast = nullptr) {
#pragma clang loop unroll(disable)
  for (int spin = 0; spin < SPIN_CAP; ++spin) {
#pragma unroll
    for (int i = 0; i < N; ++i) g[i] = gload((plast && i == N - 1) ? plast : p + (i / G) * STRIDE + i % G);
    u32 bad = 0u;
#pragma unroll
    for (int i = 0; i < N; ++i) bad |= gbad(g[i], tag);
    if (__all(bad == 0u)) break;
    if (PSLEEP) __builtin_amdgcn_s_sleep(PSLEEP);
  }
}
__device__ __forceinline__ u32 pk2(float lo, float hi) {
  bf16x2 v; v[0] = (__bf16)lo; v[1] = (__bf16)hi; return __builtin_bit_cast(u32, v);
}

__device__ __forceinline__ void pace(u32 due) {
  u32 d;
  asm volatile("s_memrealtime vcc\n\ts_waitcnt lgkmcnt(0)\n\ts_sub_u32 %0, vcc_lo, %1\n\t"
               "s_cmp_gt_i32 %0, " STR(TIMARGIN) "\n\ts_cbranch_scc1 1f\n\t"
               "s_waitcnt vmcnt(" STR(THR) ")\n"
               "2:\n\ts_cmp_ge_i32 %0, 0\n\ts_cbranch_scc1 3f\n\t"
               "s_memrealtime vcc\n\ts_waitcnt lgkmcnt(0)\n\ts_sub_u32 %0, vcc_lo, %1\n\ts_branch 2b\n"
               "1:\n\ts_waitcnt vmcnt(" STR(THR2) ")\n3:"
               : "=&s"(d) : "s"(due) : "scc", "vcc", "memory");
}
#define STAMP(i) do { } while (0)
struct Params {
  const unsigned short* x; const unsigned short* kc; const unsigned short* vc; const unsigned short* wq; const unsigned short* wo;
  const unsigned short* wg; const unsigned short* wu; const unsigned short* wd; unsigned short* out; u64* ws;
};

__global__ void __launch_bounds__(NTHR, 2) fused_decode_block(Params P) {
  __shared__ __attribute__((aligned(16))) u32 ldsu[L_TOTAL];
  float* lds = reinterpret_cast<float*>(ldsu);
  const int tid = threadIdx.x, lane = tid & 63, w = __builtin_amdgcn_readfirstlane(tid >> 6);
  const int r = lane >> 4, c = lane & 15;
  const int bid = blockIdx.x, xcd = bid & 7, slot = bid >> 3;
  const int hh = xcd * 4 + (slot & 3), b = slot >> 3, half = (slot >> 2) & 1;
  const int li = (slot & 3) * 4 + b, cg_cnt = xcd < 3 ? 6 : 5;
  const bool is_ffn = half == 1 && li < cg_cnt;
  const int cg = xcd * 5 + (xcd < 3 ? xcd : 3) + (is_ffn ? li : 0);
  gu64* ws = (gu64*)P.ws;
  const u32 sbase = (u32)(size_t)(LAS u32*)ldsu + (u32)w * 16384u;
  volatile LAS u32* flagp = (volatile LAS u32*)((LAS u32*)ldsu + L_FLAG);
  STAMP(0);

  const u32 l16 = 16u * lane;
  const u64 epoch = gload(ws + WS_EPOCH);
  const size_t kvoff = ((size_t)(b * NH + hh) * SK + (SK - WIN) + half * 1024 + 4 * w) * DH;
  const unsigned short* kp = P.kc + kvoff;
  const unsigned short* vp = P.vc + kvoff;
  constexpr int KST = 4096;
  u32x4 buf[64];
#define ISSUE(n) do { if ((n) < 64) { buf[(n) & 63] = ldnt(((n) < 32) ? kp + KST * (n) : vp + KST * ((n) - 32), l16); SB(); } } while (0)
#define TI(n) (TIEND - (63 - (n)) * TISTEP)
#define ISSUE_AT(i) do { _Pragma("unroll") for (int n_ = RA; n_ < 64; ++n_) if (sched(n_) == (i)) { \
    if (!is_ffn) pace(tstart_lo + (u32)TI(n_)); \
    ISSUE(n_); } } while (0)
  const u32 tstart_lo = (u32)__builtin_amdgcn_s_memrealtime();
#define FFN_DMA() do { \
        const int rr = lane >> 3, pc = lane & 7; \
        const u64 gaddr = (u64)((pc < 4 ? P.wg : P.wu) + (size_t)(64 * w + rr) * FF + cg * 32 + 8 * (pc & 3)); \
_Pragma("unroll") \
        for (int t = 0; t < 8; ++t) glds64(gaddr + (u64)(8 * t) * FF * 2, sbase + 1024u * t); \
        const u32 off128 = (u32)(rr * DM + 8 * pc) * 2u; \
_Pragma("unroll") \
        for (int t = 0; t < 4; ++t) glds(P.wd + (size_t)(cg * 32 + 8 * t) * DM + 64 * w, off128, sbase + 8192u + 1024u * t); \
      } while (0)
  if (tid < 2) ldsu[L_FLAG + tid] = 0u;
  u32 tag;
  if (half == 1) {
    const u32x4 xv = ld16(P.x + (w & 3) * DM, l16);
    u32x4 wqr[4];
    {
      const unsigned short* wqp = P.wq + (size_t)(128 * b + 16 * w) * QD + hh * DH;
      const u32 off = (u32)(4 * r * QD + 8 * c) * 2u;
#pragma unroll
      for (int t = 0; t < 4; ++t) wqr[t] = ldnt(wqp + (size_t)t * QD, off);
    }
    SB();
    if (FFNEARLY == 1 && is_ffn) FFN_DMA();
    SB();
#pragma unroll
    for (int i = 0; i < PACE; ++i) ISSUE(i);
    {
      float xs[8];
#pragma unroll
      for (int j = 0; j < 4; ++j) { xs[2 * j] = blo(xv[j]); xs[2 * j + 1] = bhi(xv[j]); }
      const float s = ((xs[0] + xs[1]) + (xs[2] + xs[3])) + ((xs[4] + xs[5]) + (xs[6] + xs[7]));
      const float mean = wave_sum(s) * (1.f / DM);
      float q2 = 0.f;
#pragma unroll
      for (int j = 0; j < 8; ++j) { const float d = xs[j] - mean; q2 = fmaf(d, d, q2); }
      const float var = rbf(wave_sum(q2) * (1.f / DM));
      const float mb = rbf(mean), sd = rbf(sqrtf(rbf(var + EPS)));
#pragma unroll
      for (int j = 0; j < 8; ++j) xs[j] = rbf(rbf(xs[j] - mb) / sd);
      if (w < 4 && r == b) {
        *reinterpret_cast<f32x4*>(&lds[L_XN + w * 128 + c * 8]) = f32x4{xs[0], xs[1], xs[2], xs[3]};
        *reinterpret_cast<f32x4*>(&lds[L_XN + w * 128 + c * 8 + 4]) = f32x4{xs[4], xs[5], xs[6], xs[7]};
      }
    }
    tag = (u32)__builtin_amdgcn_readfirstlane((int)epoch) + 1u;
#pragma unroll
    for (int i = PACE; i < PACE + EARLY1; ++i) ISSUE(i);
    LDS_BARRIER();
    STAMP(15);
    {
      float wf[4][8];
#pragma unroll
      for (int t = 0; t < 4; ++t)
#pragma unroll
        for (int j = 0; j < 4; ++j) { wf[t][2 * j] = blo(wqr[t][j]); wf[t][2 * j + 1] = bhi(wqr[t][j]); }
#pragma unroll
      for (int bb = 0; bb < 4; ++bb) {
        const f32x4 xn = *reinterpret_cast<const f32x4*>(&lds[L_XN + bb * 128 + 16 * w + 4 * r]);
        float qp[8];
#pragma unroll
        for (int j = 0; j < 8; ++j) qp[j] = fmaf(xn[0], wf[0][j], xn[1] * wf[1][j]) + fmaf(xn[2], wf[2][j], xn[3] * wf[3][j]);
        const float a0 = tr32(qp[0], qp[4]), a1 = tr32(qp[1], qp[5]), a2 = tr32(qp[2], qp[6]), a3 = tr32(qp[3], qp[7]);
        const float b0 = tr16(a0, a2), b1 = tr16(a1, a3);
        *reinterpret_cast<f32x2*>(&lds[L_S + w * 4096 + S_QP + bb * 128 + 8 * c + 4 * (r >> 1) + 2 * (r & 1)]) = f32x2{b0, b1};
      }
    }
    SB();
#pragma unroll
    for (int i = PACE + EARLY1; i < PRE; ++i) ISSUE(i);
    LDS_BARRIER();
    {
      float v[8];
#pragma unroll
      for (int i = 0; i < NW; ++i) v[i] = lds[L_S + i * 4096 + S_QP + tid];
      gstore(ws + WS_Q + ((size_t)(hh * NB + (tid >> 7)) * 4 + b) * DH + (tid & 127), gpack(tag, ((v[0] + v[1]) + (v[2] + v[3])) + ((v[4] + v[5]) + (v[6] + v[7]))));
    }
    SB();
    STAMP(14);
    if (FFNEARLY == 2 && is_ffn) FFN_DMA();
    SB();
  } else {
    const u32 off256 = (u32)(r * DM + 8 * c) * 2u;
#pragma unroll
    for (int t = 0; t < 4; ++t) glds(P.wo + (size_t)(hh * DH + 16 * w + 4 * t) * DM + 128 * b, off256, sbase + 1024u * t);
    SB();
#pragma unroll
    for (int i = 0; i < PACE; ++i) ISSUE(i);
    tag = (u32)__builtin_amdgcn_readfirstlane((int)epoch) + 1u;
    LDS_BARRIER();
#pragma unroll
    for (int i = PACE; i < PRE; ++i) { asm volatile("s_waitcnt vmcnt(" STR(PACE) "-1)" ::: "memory"); ISSUE(i); }
  }
  if (w < 2) {
    u64 g[4];
    sweep<4, DH>(ws + WS_Q + (size_t)(hh * NB + b) * 4 * DH + tid, g, tag);
    const u32 pk = pair_pack(rbf((gval(g[0]) + gval(g[1])) + (gval(g[2]) + gval(g[3]))));
    if ((lane & 1) == 0) ldsu[L_QBF + (tid >> 1)] = pk;
    LGKM0();
    if (lane == 0) flagp[w] = 1u;
    STAMP(17);
  }
  SB();
  {
    bool ready = w < 2;
#pragma unroll
    for (int i = PRE; i < RA; ++i) {
      if (!ready) {
        const u32x2 fl = *(volatile LAS u32x2*)flagp;
        ready = __builtin_amdgcn_readfirstlane(fl[0] & fl[1]) != 0;
        if (!ready) asm volatile("s_waitcnt vmcnt(" STR(PACE) "-1)" ::: "memory");
      }
      ISSUE(i);
    }
#pragma clang loop unroll(disable)
    for (int spin = 0; spin < (SPIN_CAP << 4); ++spin) {
      const u32x2 fl = *(volatile LAS u32x2*)flagp;
      if (__builtin_amdgcn_readfirstlane(fl[0] & fl[1]) != 0) break;
    }
    MEMFENCE();
  }
  const u32x4 qv = *reinterpret_cast<const u32x4*>(&ldsu[L_QBF + 4 * c]);
  STAMP(1);

  float sc0 = 0.f, sc1 = 0.f;
  {
    float xd[16];
#define TR_STEP(HALF, CTRL, BIT) _Pragma("unroll") for (int h_ = 0; h_ < (HALF); ++h_) { \
    const float keep_ = (c & (BIT)) ? xd[h_ + (HALF)] : xd[h_], send_ = (c & (BIT)) ? xd[h_] : xd[h_ + (HALF)]; \
    xd[h_] = keep_ + dpp<CTRL>(send_); }
#pragma unroll
    for (int i = 0; i < 32; ++i) {
      const u32x4 kv = buf[i];
      float d = dot2(kv[0], qv[0], 0.f); d = dot2(kv[1], qv[1], d); d = dot2(kv[2], qv[2], d); d = dot2(kv[3], qv[3], d);
      asm volatile("" : "+v"(d));
      xd[i & 15] = d;
      ISSUE_AT(i);
      SB();
      if (LOCKK && i < 31 && (i % LOCKK) == LOCKK - 1) __builtin_amdgcn_s_barrier();
      if ((i & 15) == 15) {
        TR_STEP(8, ROR8, 8) TR_STEP(4, XOR7, 4) TR_STEP(2, XOR2, 2) TR_STEP(1, XOR1, 1)
        const float s = rbf(rbf(xd[0]) * SCALE);
        if (i < 16) sc0 = s; else sc1 = s;
        SB();
      }
    }
#undef TR_STEP
  }
  {
    const float wm = wave_max(fmaxf(sc0, sc1));
    if (lane == 0) lds[L_WMAX + w] = wm;
  }
  LDS_BARRIER();
  float Mb;
  {
    const f32x4 m0 = *reinterpret_cast<const f32x4*>(&lds[L_WMAX]), m1 = *reinterpret_cast<const f32x4*>(&lds[L_WMAX + 4]);
    Mb = fmaxf(fmaxf(fmaxf(m0[0], m0[1]), fmaxf(m0[2], m0[3])), fmaxf(fmaxf(m1[0], m1[1]), fmaxf(m1[2], m1[3])));
  }
  const float p0 = __builtin_amdgcn_exp2f((sc0 - Mb) * LOG2E), p1 = __builtin_amdgcn_exp2f((sc1 - Mb) * LOG2E);
  {
    const float ls = wave_sum(p0 + p1);
    if (lane == 0) lds[L_WSUM + w] = ls;
  }
  const int fr_row = 8 * r + ((lane >> 2) & 3), fr_col = (lane & 3) * 2;
  u32x4 wof[4];
#pragma unroll
  for (int s = 0; s < 4; ++s) wof[s] = u32x4{0u, 0u, 0u, 0u};
  if (half == 0) {
#pragma unroll
    for (int s = 0; s < 4; ++s)
      wof[s] = bfrag<64>(ldsu + L_S + (2 * s + (fr_row >> 4)) * 4096 + (fr_row & 15) * 64 + w * 8 + fr_col);
  }
  STAMP(2);

  float acc[8];
#pragma unroll
  for (int j = 0; j < 8; ++j) acc[j] = 0.f;
  float Lb = 0.f;
  u64 gpm = 0, gpl = 0;
  gu64* pml = ws + WS_ML + (size_t)((b * NH + hh) * 2 + (half ^ 1)) * 2;
#define V_STEP(i) { const u32x4 vv = buf[32 + (i)]; const float pi = dpp<0x150 + ((i) & 15)>((i) < 16 ? p0 : p1); \
    _Pragma("unroll") for (int j = 0; j < 4; ++j) { acc[2 * j] = fmaf(pi, blo(vv[j]), acc[2 * j]); acc[2 * j + 1] = fmaf(pi, bhi(vv[j]), acc[2 * j + 1]); } \
    asm volatile("" : "+v"(acc[0]), "+v"(acc[1]), "+v"(acc[2]), "+v"(acc[3]), "+v"(acc[4]), "+v"(acc[5]), "+v"(acc[6]), "+v"(acc[7])); \
    ISSUE_AT(32 + (i)); SB(); if (LOCKV && (i) > 3 && (i) < LOCKVEND && ((i) % LOCKV) == LOCKV - 1) __builtin_amdgcn_s_barrier(); }
  V_STEP(0) V_STEP(1) V_STEP(2) V_STEP(3)
  LDS_BARRIER();
  {
    const f32x4 s0 = *reinterpret_cast<const f32x4*>(&lds[L_WSUM]), s1 = *reinterpret_cast<const f32x4*>(&lds[L_WSUM + 4]);
    Lb = ((s0[0] + s0[1]) + (s0[2] + s0[3])) + ((s1[0] + s1[1]) + (s1[2] + s1[3]));
    if (w == 0 && lane < 2) gstore(ws + WS_ML + (size_t)((b * NH + hh) * 2 + half) * 2 + lane, gpack(tag, lane == 0 ? Mb : Lb));
  }
  SB();
  V_STEP(4) V_STEP(5) V_STEP(6) V_STEP(7) V_STEP(8) V_STEP(9) V_STEP(10) V_STEP(11) V_STEP(12) V_STEP(13) V_STEP(14) V_STEP(15)
  gpm = gload(pml); gpl = gload(pml + 1);
  SB();
  V_STEP(16) V_STEP(17) V_STEP(18) V_STEP(19) V_STEP(20) V_STEP(21) V_STEP(22) V_STEP(23) V_STEP(24) V_STEP(25) V_STEP(26) V_STEP(27)
  static_assert(sched(63) <= 32 + 27, "all stream loads are issued before the tail loop");
  asm volatile("" : "+v"(gpm), "+v"(gpl));
#pragma clang loop unroll(disable)
  for (int spin = 0; spin < SPIN_CAP; ++spin) {
    if ((gbad(gpm, tag) | gbad(gpl, tag)) == 0u) break;
    gpm = gload(pml); gpl = gload(pml + 1);
  }
  float fsc;
  {
    const float m2 = gval(gpm), l2 = gval(gpl);
    const float M = fmaxf(Mb, m2), e = __builtin_amdgcn_exp2f((Mb - M) * LOG2E), e2 = __builtin_amdgcn_exp2f((m2 - M) * LOG2E);
    fsc = e * __builtin_amdgcn_rcpf(fmaf(Lb, e, l2 * e2));
  }
  const int mycol = 16 * hh + c;
  gu64* odst = ws + WS_O + ((size_t)(hh * 2 + half) * NB + b) * DH + tid;
  asm volatile("" : "+v"(odst), "+v"(fsc));
  SB();
  {
    V_STEP(28) V_STEP(29) V_STEP(30) V_STEP(31)
    STAMP(3);
    {
      const float f = fsc;
      const float a0 = tr32(acc[0], acc[4]), a1 = tr32(acc[1], acc[5]), a2 = tr32(acc[2], acc[6]), a3 = tr32(acc[3], acc[7]);
      const float b0 = tr16(a0, a2) * f, b1 = tr16(a1, a3) * f;
      *reinterpret_cast<f32x2*>(&lds[L_PART + w * 128 + 8 * c + 4 * (r >> 1) + 2 * (r & 1)]) = f32x2{b0, b1};
    }
    LDS_BARRIER();
    if (w < 2) {
      float v[8];
#pragma unroll
      for (int i = 0; i < NW; ++i) v[i] = lds[L_PART + i * 128 + tid];
      const u64 og = gpack(tag, ((v[0] + v[1]) + (v[2] + v[3])) + ((v[4] + v[5]) + (v[6] + v[7])));
      gstore(odst, og);
    }
    SB();
    STAMP(4);
    if (half == 0) {
      const u32 xraw = (u32)P.x[b * DM + mycol];
      gu64* ydst = ws + WS_Y + (((size_t)(r * 32 + 8 * b + w)) * NH + hh) * 16 + c;
      asm volatile("" : "+v"(ydst));
      {
        u64 g[2] = {0ull, 0ull};
        sweep<2, NB * DH>(ws + WS_O + (size_t)(hh * 2) * NB * DH + tid, g, tag);
        const u32 pk = pair_pack(rbf(gval(g[0]) + gval(g[1])));
        if ((lane & 1) == 0) ldsu[L_ACT + (tid >> 7) * P_O + ((tid & 127) >> 1)] = pk;
      }
      LDS_BARRIER();
      STAMP(13);
      {
        u32x4 af[4];
#pragma unroll
        for (int s = 0; s < 4; ++s) af[s] = *reinterpret_cast<const u32x4*>(&ldsu[L_ACT + (lane & 3) * P_O + 16 * s + 4 * r]);
        SB();
        f32x4 d = mfma(af[0], wof[0], f32x4{0.f, 0.f, 0.f, 0.f}), d2 = mfma(af[1], wof[1], f32x4{0.f, 0.f, 0.f, 0.f});
        d = mfma(af[2], wof[2], d); d2 = mfma(af[3], wof[3], d2);
        d += d2;
        const u64 yg = gpack(tag, sel4(r, d[0], d[1], d[2], d[3]));
        gstore(ydst, yg);
      }
      STAMP(16);
      if (w == 0) {
        u64 y[8];
        sweep<8, 64>(ws + WS_Y + (size_t)(b * 32 + hh) * (NH * 16) + lane, y, tag);
        STAMP(9);
        float v = ((gval(y[0]) + gval(y[1])) + (gval(y[2]) + gval(y[3]))) + ((gval(y[4]) + gval(y[5])) + (gval(y[6]) + gval(y[7])));
        v = tr16(v, v); v = tr32(v, v);
        const float hval = rbf(blo(xraw) + rbf(v));
        if (lane < 16) gstore(ws + WS_H + b * DM + mycol, gpack(tag, hval));
        STAMP(5);
        gu64* zp = ws + WS_Z + (size_t)(b * 32 + hh) * (NCG * 16);
        const bool live = lane < NCG * 16 - 640;
        u64 g[11];
        sweep<11, 64>(zp + lane, g, tag, zp + (live ? 640 + lane : lane));
        STAMP(12);
        float z = (((gval(g[0]) + gval(g[1])) + (gval(g[2]) + gval(g[3]))) + ((gval(g[4]) + gval(g[5])) + (gval(g[6]) + gval(g[7])))) +
                  ((gval(g[8]) + gval(g[9])) + (live ? gval(g[10]) : 0.f));
        z = tr16(z, z); z = tr32(z, z);
        const float o = rbf(hval + rbf(z));
        {
          if (lane < 16) P.out[b * DM + mycol] = (unsigned short)(__float_as_uint(o) >> 16);
          if (bid == 0 && lane == 0) gstore(ws + WS_EPOCH, epoch + 1ull);
        }
      }
    } else if (is_ffn) {
      if (FFNEARLY == 0) { FFN_DMA(); asm volatile("s_waitcnt vmcnt(0)" ::: "memory"); }
      LDS_BARRIER();
      u32x4 guf[16], wdf[4];
      const int gcol = ((lane & 2) ? 16 : 0) + (w & 3) * 4 + (lane & 1) * 2;
#pragma unroll
      for (int s = 0; s < 16; ++s) guf[s] = bfrag<32>(ldsu + L_S + (s >> 1) * 4096 + (s & 1) * 1024 + fr_row * 32 + gcol);
#pragma unroll
      for (int t = 0; t < 4; ++t) wdf[t] = bfrag<32>(ldsu + L_S + w * 4096 + 2048 + fr_row * 32 + t * 8 + fr_col);
      gu64* zdst = ws + WS_Z + (((size_t)(4 * w + r)) * NCG + cg) * 16 + c;
      asm volatile("" : "+v"(zdst));
      STAMP(18);
      if (w < 4) {
        u64 g[8];
        sweep<8, 128, 2>(ws + WS_H + w * DM + 2 * lane, g, tag);
        STAMP(10);
        float h[8];
#pragma unroll
        for (int i = 0; i < 8; ++i) h[i] = gval(g[i]);
        const float mean = wave_sum(((h[0] + h[1]) + (h[2] + h[3])) + ((h[4] + h[5]) + (h[6] + h[7]))) * (1.f / DM);
        float q0 = 0.f, q1 = 0.f;
#pragma unroll
        for (int i = 0; i < 4; ++i) { const float d0 = h[i] - mean, d1 = h[i + 4] - mean; q0 = fmaf(d0, d0, q0); q1 = fmaf(d1, d1, q1); }
        const float mb = rbf(mean);
        u32 dk[4];
#pragma unroll
        for (int j = 0; j < 4; ++j) dk[j] = pk2(h[2 * j] - mb, h[2 * j + 1] - mb);
        const float var = rbf(wave_sum(q0 + q1) * (1.f / DM));
        const float inv = __builtin_amdgcn_rcpf(rbf(__builtin_amdgcn_sqrtf(rbf(var + EPS))));
#pragma unroll
        for (int j = 0; j < 4; ++j) ldsu[L_HN + w * P_HN + 64 * j + lane] = pk2(blo(dk[j]) * inv, bhi(dk[j]) * inv);
      }
      LDS_BARRIER();
      STAMP(6);
      if (w < 4) {
        u32x4 af[16];
#pragma unroll
        for (int s = 0; s < 16; ++s) af[s] = *reinterpret_cast<const u32x4*>(&ldsu[L_HN + (lane & 3) * P_HN + 16 * s + 4 * r]);
        SB();
        f32x4 d0 = {0.f, 0.f, 0.f, 0.f}, d1 = {0.f, 0.f, 0.f, 0.f};
#pragma unroll
        for (int s = 0; s < 16; s += 2) { d0 = mfma(af[s], guf[s], d0); d1 = mfma(af[s + 1], guf[s + 1], d1); }
        const float v = rbf(sel4(r, d0[0] + d1[0], d0[1] + d1[1], d0[2] + d1[2], d0[3] + d1[3]));
        const float uu = dpp<ROR8>(v);
        const float sg = rbf(v * __builtin_amdgcn_rcpf(1.f + __builtin_amdgcn_exp2f(-v * LOG2E)));
        const u32 pk = pair_pack(rbf(sg * uu));
        if (c < 8 && (lane & 1) == 0) ldsu[L_A2 + r * 16 + 4 * w + (c >> 1)] = pk;
      }
      LDS_BARRIER();
      STAMP(11);
      {
        const u32x4 a = *reinterpret_cast<const u32x4*>(&ldsu[L_A2 + (lane & 3) * 16 + 4 * r]);
        f32x4 d[4];
#pragma unroll
        for (int t = 0; t < 4; ++t) d[t] = mfma(a, wdf[t], f32x4{0.f, 0.f, 0.f, 0.f});
#pragma unroll
        for (int i = 0; i < 4; ++i) {
          const u64 zg = gpack(tag, sel4(r, d[0][i], d[1][i], d[2][i], d[3][i]));
          gstore(zdst + (size_t)i * (32 * NCG * 16), zg);
        }
      }
      STAMP(7);
    }
  }
#undef V_STEP
}

extern "C" void kernel_launch(void* const* d_in, const int* in_sizes, int n_in, void* d_out, int out_size, void* d_ws, size_t ws_size,
                              hipStream_t stream) {
  Params P;
  P.x = (const unsigned short*)d_in[0]; P.kc = (const unsigned short*)d_in[1]; P.vc = (const unsigned short*)d_in[2];
  P.wq = (const unsigned short*)d_in[3]; P.wo = (const unsigned short*)d_in[4];
  P.wg = (const unsigned short*)d_in[6]; P.wu = (const unsigned short*)d_in[7]; P.wd = (const unsigned short*)d_in[8];
  P.out = (unsigned short*)d_out; P.ws = (u64*)d_ws;
  hipLaunchKernelGGL(fused_decode_block, dim3(256), dim3(NTHR), 0, stream, P);
}
```

```cpp
#include <hip/hip_runtime.h>
#include <hip/hip_bf16.h>
#include <stdint.h>

typedef unsigned u32;
typedef unsigned long long u64;
typedef u32 u32x4 __attribute__((ext_vector_type(4)));
typedef u32 u32x2 __attribute__((ext_vector_type(2)));
typedef float f32x4 __attribute__((ext_vector_type(4)));
typedef float f32x2 __attribute__((ext_vector_type(2)));
typedef short s16x4 __attribute__((ext_vector_type(4)));
typedef __bf16 bf16x2 __attribute__((ext_vector_type(2)));
typedef __bf16 bf16x8 __attribute__((ext_vector_type(8)));
#define GAS __attribute__((address_space(1)))
#define LAS __attribute__((address_space(3)))
typedef GAS u64 gu64;
#define RLX_AGENT __ATOMIC_RELAXED, __HIP_MEMORY_SCOPE_AGENT

constexpr int NB = 4, DM = 512, NH = 32, SK = 8192, DH = 128, WIN = 2048, FF = 1376, QD = NH * DH;
constexpr int NCG = FF / 32;
constexpr int NTHR = 512, NW = 8;
#define PACE 3
#define THR 2
#define THR2 4
constexpr int THRN = 52, DIST = 12, RUNAHEAD = 28, EARLY1 = 3, EARLY2 = 3, LOCK = 2, DSTEP = 4;
constexpr int PRE = PACE + EARLY1 + EARLY2;
constexpr int RA = RUNAHEAD;
constexpr int sched(int n) { return 2 * (n - RA) < n - DIST ? 2 * (n - RA) : n - DIST; }
constexpr float SCALE = 0.08838834764831845f;
constexpr float LOG2E = 1.4426950408889634f;
constexpr float EPS = 1e-5f;
constexpr int SPIN_CAP = 1 << 18;
#define STR2(x) #x
#define STR(x) STR2(x)

constexpr size_t WS_EPOCH = 0;
constexpr size_t WS_ML = 128;
constexpr size_t WS_H = WS_ML + NB * NH * 4;
constexpr size_t WS_Q = WS_H + NB * DM;
constexpr size_t WS_O = WS_Q + (size_t)NH * NB * 4 * DH;
constexpr size_t WS_Y = WS_O + (size_t)NH * 8 * DH;
constexpr size_t WS_Z = WS_Y + (size_t)NB * 32 * NH * 16;

constexpr int L_S = 0;
constexpr int S_QP = 3072;
constexpr int L_PART = L_S + NW * 4096;
constexpr int L_QBF = L_PART + NW * 128;
constexpr int L_XN = L_QBF + 64;
constexpr int L_WMAX = L_XN + 512;
constexpr int L_WSUM = L_WMAX + 8;
constexpr int P_O = 80, P_HN = 272;
constexpr int L_ACT = L_WSUM + 8;
constexpr int L_HN = L_ACT + 4 * P_O;
constexpr int L_A2 = L_HN + 4 * P_HN;
constexpr int L_STAT = L_A2 + 64;
constexpr int L_RED = L_STAT + 64;
constexpr int L_FLAG = L_RED + NW * 256;
constexpr int L_TOTAL = L_FLAG + 4;

__device__ __forceinline__ float rbf(float x) { return (float)(__bf16)x; }
__device__ __forceinline__ float blo(u32 u) { return __builtin_bit_cast(float, u << 16); }
__device__ __forceinline__ float bhi(u32 u) { return __builtin_bit_cast(float, u & 0xffff0000u); }
__device__ __forceinline__ float dot2(u32 a, u32 b, float acc) {
  return __builtin_amdgcn_fdot2_f32_bf16(__builtin_bit_cast(bf16x2, a), __builtin_bit_cast(bf16x2, b), acc, false);
}
template <int CTRL> __device__ __forceinline__ float dpp(float x) {
  return __builtin_bit_cast(float, __builtin_amdgcn_mov_dpp(__builtin_bit_cast(int, x), CTRL, 0xf, 0xf, true));
}
constexpr int XOR1 = 0xB1, XOR2 = 0x4E, XOR7 = 0x141, ROR8 = 0x128, ROR4 = 0x124;
__device__ __forceinline__ float tr32(float a, float b) {
  auto s = __builtin_amdgcn_permlane32_swap(__float_as_uint(a), __float_as_uint(b), false, false);
  return __uint_as_float(s[0]) + __uint_as_float(s[1]);
}
__device__ __forceinline__ float tr16(float a, float b) {
  auto s = __builtin_amdgcn_permlane16_swap(__float_as_uint(a), __float_as_uint(b), false, false);
  return __uint_as_float(s[0]) + __uint_as_float(s[1]);
}
__device__ __forceinline__ float wave_sum(float v) {
  v += dpp<XOR1>(v); v += dpp<XOR2>(v); v += dpp<XOR7>(v); v += dpp<ROR8>(v);
  v = tr16(v, v); v = tr32(v, v); return v;
}
__device__ __forceinline__ float wave_max(float v) {
  v = fmaxf(v, dpp<XOR1>(v)); v = fmaxf(v, dpp<XOR2>(v)); v = fmaxf(v, dpp<XOR7>(v)); v = fmaxf(v, dpp<ROR8>(v));
  auto s = __builtin_amdgcn_permlane16_swap(__float_as_uint(v), __float_as_uint(v), false, false);
  v = fmaxf(__uint_as_float(s[0]), __uint_as_float(s[1]));
  auto t = __builtin_amdgcn_permlane32_swap(__float_as_uint(v), __float_as_uint(v), false, false);
  return fmaxf(__uint_as_float(t[0]), __uint_as_float(t[1]));
}
__device__ __forceinline__ u32 pair_pack(float v) { return (__float_as_uint(v) >> 16) | (__float_as_uint(dpp<XOR1>(v)) & 0xffff0000u); }
__device__ __forceinline__ u32x4 ldnt(const void* base, u32 off) { return __builtin_nontemporal_load(reinterpret_cast<const u32x4*>((const char*)base + off)); }
__device__ __forceinline__ u32x4 ld16(const void* base, u32 off) { return *reinterpret_cast<const u32x4*>((const char*)base + off); }
__device__ __forceinline__ void glds(const void* base, u32 off, u32 lds_dst) {
  u32 keep;
  asm volatile("s_mov_b32 %0, m0\n\ts_mov_b32 m0, %3\n\ts_nop 0\n\tglobal_load_lds_dwordx4 %1, %2 nt\n\ts_mov_b32 m0, %0"
               : "=&s"(keep) : "v"(off), "s"(base), "s"(lds_dst) : "memory");
}
__device__ __forceinline__ void glds64(u64 addr, u32 lds_dst) {
  u32 keep;
  asm volatile("s_mov_b32 %0, m0\n\ts_mov_b32 m0, %2\n\ts_nop 0\n\tglobal_load_lds_dwordx4 %1, off nt\n\ts_mov_b32 m0, %0"
               : "=&s"(keep) : "v"(addr), "s"(lds_dst) : "memory");
}
template <int PITCH> __device__ __forceinline__ u32x4 bfrag(const u32* p) {
  const u32x2 a = __builtin_bit_cast(u32x2, __builtin_amdgcn_ds_read_tr16_b64_v4i16((LAS s16x4*)p));
  const u32x2 b = __builtin_bit_cast(u32x2, __builtin_amdgcn_ds_read_tr16_b64_v4i16((LAS s16x4*)(p + 4 * PITCH)));
  return u32x4{a[0], a[1], b[0], b[1]};
}
__device__ __forceinline__ f32x4 mfma(u32x4 a, u32x4 b, f32x4 c) {
  return __builtin_amdgcn_mfma_f32_16x16x32_bf16(__builtin_bit_cast(bf16x8, a), __builtin_bit_cast(bf16x8, b), c, 0, 0, 0);
}
__device__ __forceinline__ float sel4(int g, float a, float b, float c, float d) { return g & 2 ? (g & 1 ? d : c) : (g & 1 ? b : a); }
#define SB() __builtin_amdgcn_sched_barrier(0)
#define MEMFENCE() asm volatile("" ::: "memory")
#define LDS_BARRIER() asm volatile("s_waitcnt lgkmcnt(0)\n\ts_barrier" ::: "memory")
#define LGKM0() asm volatile("s_waitcnt lgkmcnt(0)" ::: "memory")

__device__ __forceinline__ u64 gpack(u32 tag, float v) { return ((u64)tag << 32) | (u64)__float_as_uint(v); }
__device__ __forceinline__ float gval(u64 g) { return __uint_as_float((u32)g); }
__device__ __forceinline__ u32 gbad(u64 g, u32 tag) { return (u32)(g >> 32) ^ tag; }
__device__ __forceinline__ void gstore(gu64* p, u64 v) { __hip_atomic_store(p, v, RLX_AGENT); }
__device__ __forceinline__ u64 gload(gu64* p) { return __hip_atomic_load(p, RLX_AGENT); }
template <int N, int STRIDE, int G = 1> __device__ __forceinline__ void sweep(gu64* p, u64 (&g)[N], u32 tag, gu64* plast = nullptr) {
#pragma clang loop unroll(disable)
  for (int spin = 0; spin < SPIN_CAP; ++spin) {
#pragma unroll
    for (int i = 0; i < N; ++i) g[i] = gload((plast && i == N - 1) ? plast : p + (i / G) * STRIDE + i % G);
    u32 bad = 0u;
#pragma unroll
    for (int i = 0; i < N; ++i) bad |= gbad(g[i], tag);
    if (__all(bad == 0u)) break;
  }
}
__device__ __forceinline__ u32 pk2(float lo, float hi) {
  bf16x2 v; v[0] = (__bf16)lo; v[1] = (__bf16)hi; return __builtin_bit_cast(u32, v);
}

struct Params {
  const unsigned short* x; const unsigned short* kc; const unsigned short* vc; const unsigned short* wq; const unsigned short* wo;
  const unsigned short* wg; const unsigned short* wu; const unsigned short* wd; unsigned short* out; u64* ws;
};

__global__ void __launch_bounds__(NTHR, 2) fused_decode_block(Params P) {
  __shared__ __attribute__((aligned(16))) u32 ldsu[L_TOTAL];
  float* lds = reinterpret_cast<float*>(ldsu);
  const int tid = threadIdx.x, lane = tid & 63, w = __builtin_amdgcn_readfirstlane(tid >> 6);
  const int r = lane >> 4, c = lane & 15;
  const int bid = blockIdx.x, xcd = bid & 7, slot = bid >> 3;
  const int hh = xcd * 4 + (slot & 3), b = slot >> 3, half = (slot >> 2) & 1;
  const int li = (slot & 3) * 4 + b, cg_cnt = xcd < 3 ? 6 : 5;
  const bool is_ffn = half == 1 && li < cg_cnt;
  const int cg = xcd * 5 + (xcd < 3 ? xcd : 3) + (is_ffn ? li : 0);
  gu64* ws = (gu64*)P.ws;
  const u32 sbase = (u32)(size_t)(LAS u32*)ldsu + (u32)w * 16384u;
  volatile LAS u32* flagp = (volatile LAS u32*)((LAS u32*)ldsu + L_FLAG);

  const u32 l16 = 16u * lane;
  const u64 epoch = gload(ws + WS_EPOCH);
  const size_t kvoff = ((size_t)(b * NH + hh) * SK + (SK - WIN) + half * 1024 + 4 * w) * DH;
  const unsigned short* kp = P.kc + kvoff;
  const unsigned short* vp = P.vc + kvoff;
  constexpr int KST = 4096;
  u32x4 buf[64];
#define ISSUE(n) do { if ((n) < 64) { buf[(n) & 63] = ldnt(((n) < 32) ? kp + KST * (n) : vp + KST * ((n) - 32), l16); SB(); } } while (0)
#define ISSUE_AT(i) do { _Pragma("unroll") for (int n_ = RA; n_ < 64; ++n_) if (sched(n_) == (i)) { \
    if (!is_ffn) { if (n_ < THRN) asm volatile("s_waitcnt vmcnt(" STR(THR) ")" ::: "memory"); else asm volatile("s_waitcnt vmcnt(" STR(THR2) ")" ::: "memory"); } \
    ISSUE(n_); } } while (0)
#define FFN_DMA_GU() do { \
    const int rr = lane >> 3, pc = lane & 7; \
    const u64 gaddr = (u64)((pc < 4 ? P.wg : P.wu) + (size_t)(64 * w + rr) * FF + cg * 32 + 8 * (pc & 3)); \
    _Pragma("unroll") for (int t = 0; t < 8; ++t) glds64(gaddr + (u64)(8 * t) * FF * 2, sbase + 1024u * t); } while (0)
#define FFN_DMA_D() do { \
    const u32 off128 = (u32)((lane >> 3) * DM + 8 * (lane & 7)) * 2u; \
    _Pragma("unroll") for (int t = 0; t < 4; ++t) glds(P.wd + (size_t)(cg * 32 + 8 * t) * DM + 64 * w, off128, sbase + 8192u + 1024u * t); } while (0)
  if (tid < 2) ldsu[L_FLAG + tid] = 0u;
  u32 tag;
  if (half == 1) {
    const u32x4 xv = ld16(P.x + (w & 3) * DM, l16);
    u32x4 wqr[4];
    {
      const unsigned short* wqp = P.wq + (size_t)(128 * b + 16 * w) * QD + hh * DH;
      const u32 off = (u32)(4 * r * QD + 8 * c) * 2u;
#pragma unroll
      for (int t = 0; t < 4; ++t) wqr[t] = ldnt(wqp + (size_t)t * QD, off);
    }
    SB();
#pragma unroll
    for (int i = 0; i < PACE; ++i) ISSUE(i);
    {
      float xs[8];
#pragma unroll
      for (int j = 0; j < 4; ++j) { xs[2 * j] = blo(xv[j]); xs[2 * j + 1] = bhi(xv[j]); }
      const float s = ((xs[0] + xs[1]) + (xs[2] + xs[3])) + ((xs[4] + xs[5]) + (xs[6] + xs[7]));
      const float mean = wave_sum(s) * (1.f / DM);
      float q2 = 0.f;
#pragma unroll
      for (int j = 0; j < 8; ++j) { const float d = xs[j] - mean; q2 = fmaf(d, d, q2); }
      const float var = rbf(wave_sum(q2) * (1.f / DM));
      const float mb = rbf(mean), sd = rbf(sqrtf(rbf(var + EPS)));
#pragma unroll
      for (int j = 0; j < 8; ++j) xs[j] = rbf(rbf(xs[j] - mb) / sd);
      if (w < 4 && r == b) {
        *reinterpret_cast<f32x4*>(&lds[L_XN + w * 128 + c * 8]) = f32x4{xs[0], xs[1], xs[2], xs[3]};
        *reinterpret_cast<f32x4*>(&lds[L_XN + w * 128 + c * 8 + 4]) = f32x4{xs[4], xs[5], xs[6], xs[7]};
      }
    }
    tag = (u32)__builtin_amdgcn_readfirstlane((int)epoch) + 1u;
#pragma unroll
    for (int i = PACE; i < PACE + EARLY1; ++i) ISSUE(i);
    LDS_BARRIER();
    {
      float wf[4][8];
#pragma unroll
      for (int t = 0; t < 4; ++t)
#pragma unroll
        for (int j = 0; j < 4; ++j) { wf[t][2 * j] = blo(wqr[t][j]); wf[t][2 * j + 1] = bhi(wqr[t][j]); }
#pragma unroll
      for (int bb = 0; bb < 4; ++bb) {
        const f32x4 xn = *reinterpret_cast<const f32x4*>(&lds[L_XN + bb * 128 + 16 * w + 4 * r]);
        float qp[8];
#pragma unroll
        for (int j = 0; j < 8; ++j) qp[j] = fmaf(xn[0], wf[0][j], xn[1] * wf[1][j]) + fmaf(xn[2], wf[2][j], xn[3] * wf[3][j]);
        const float a0 = tr32(qp[0], qp[4]), a1 = tr32(qp[1], qp[5]), a2 = tr32(qp[2], qp[6]), a3 = tr32(qp[3], qp[7]);
        const float b0 = tr16(a0, a2), b1 = tr16(a1, a3);
        *reinterpret_cast<f32x2*>(&lds[L_S + w * 4096 + S_QP + bb * 128 + 8 * c + 4 * (r >> 1) + 2 * (r & 1)]) = f32x2{b0, b1};
      }
    }
    SB();
#pragma unroll
    for (int i = PACE + EARLY1; i < PRE; ++i) ISSUE(i);
    LDS_BARRIER();
    {
      float v[8];
#pragma unroll
      for (int i = 0; i < NW; ++i) v[i] = lds[L_S + i * 4096 + S_QP + tid];
      gstore(ws + WS_Q + ((size_t)(hh * NB + (tid >> 7)) * 4 + b) * DH + (tid & 127), gpack(tag, ((v[0] + v[1]) + (v[2] + v[3])) + ((v[4] + v[5]) + (v[6] + v[7]))));
    }
    SB();
  } else {
    const u32 off256 = (u32)(r * DM + 8 * c) * 2u;
#pragma unroll
    for (int t = 0; t < 4; ++t) glds(P.wo + (size_t)(hh * DH + 16 * w + 4 * t) * DM + 128 * b, off256, sbase + 1024u * t);
    SB();
#pragma unroll
    for (int i = 0; i < PACE; ++i) ISSUE(i);
    tag = (u32)__builtin_amdgcn_readfirstlane((int)epoch) + 1u;
    LDS_BARRIER();
#pragma unroll
    for (int i = PACE; i < PRE; ++i) { asm volatile("s_waitcnt vmcnt(" STR(PACE) "-1)" ::: "memory"); ISSUE(i); }
  }
  if (w < 2) {
    u64 g[4];
    sweep<4, DH>(ws + WS_Q + (size_t)(hh * NB + b) * 4 * DH + tid, g, tag);
    const u32 pk = pair_pack(rbf((gval(g[0]) + gval(g[1])) + (gval(g[2]) + gval(g[3]))));
    if ((lane & 1) == 0) ldsu[L_QBF + (tid >> 1)] = pk;
    LGKM0();
    if (lane == 0) flagp[w] = 1u;
  }
  SB();
  {
    bool ready = w < 2;
#pragma unroll
    for (int i = PRE; i < RA; ++i) {
      if (!ready) {
        const u32x2 fl = *(volatile LAS u32x2*)flagp;
        ready = __builtin_amdgcn_readfirstlane(fl[0] & fl[1]) != 0;
        if (!ready) asm volatile("s_waitcnt vmcnt(" STR(PACE) "-1)" ::: "memory");
      }
      ISSUE(i);
    }
#pragma clang loop unroll(disable)
    for (int spin = 0; spin < (SPIN_CAP << 4); ++spin) {
      const u32x2 fl = *(volatile LAS u32x2*)flagp;
      if (__builtin_amdgcn_readfirstlane(fl[0] & fl[1]) != 0) break;
    }
    MEMFENCE();
  }
  const u32x4 qv = *reinterpret_cast<const u32x4*>(&ldsu[L_QBF + 4 * c]);

  float sc0 = 0.f, sc1 = 0.f;
  {
    float xd[16];
#define TR_STEP(HALF, CTRL, BIT) _Pragma("unroll") for (int h_ = 0; h_ < (HALF); ++h_) { \
    const float keep_ = (c & (BIT)) ? xd[h_ + (HALF)] : xd[h_], send_ = (c & (BIT)) ? xd[h_] : xd[h_ + (HALF)]; \
    xd[h_] = keep_ + dpp<CTRL>(send_); }
#pragma unroll
    for (int i = 0; i < 32; ++i) {
      const u32x4 kv = buf[i];
      float d = dot2(kv[0], qv[0], 0.f); d = dot2(kv[1], qv[1], d); d = dot2(kv[2], qv[2], d); d = dot2(kv[3], qv[3], d);
      asm volatile("" : "+v"(d));
      xd[i & 15] = d;
      ISSUE_AT(i);
      SB();
      if (i == DSTEP && is_ffn) { FFN_DMA_D(); SB(); }
      if (i < 31 && (i % LOCK) == LOCK - 1) __builtin_amdgcn_s_barrier();
      if ((i & 15) == 15) {
        TR_STEP(8, ROR8, 8) TR_STEP(4, XOR7, 4) TR_STEP(2, XOR2, 2) TR_STEP(1, XOR1, 1)
        const float s = rbf(rbf(xd[0]) * SCALE);
        if (i < 16) sc0 = s; else sc1 = s;
        SB();
      }
    }
#undef TR_STEP
  }
  {
    const float wm = wave_max(fmaxf(sc0, sc1));
    if (lane == 0) lds[L_WMAX + w] = wm;
  }
  LDS_BARRIER();
  float Mb;
  {
    const f32x4 m0 = *reinterpret_cast<const f32x4*>(&lds[L_WMAX]), m1 = *reinterpret_cast<const f32x4*>(&lds[L_WMAX + 4]);
    Mb = fmaxf(fmaxf(fmaxf(m0[0], m0[1]), fmaxf(m0[2], m0[3])), fmaxf(fmaxf(m1[0], m1[1]), fmaxf(m1[2], m1[3])));
  }
  const float p0 = __builtin_amdgcn_exp2f((sc0 - Mb) * LOG2E), p1 = __builtin_amdgcn_exp2f((sc1 - Mb) * LOG2E);
  {
    const float ls = wave_sum(p0 + p1);
    if (lane == 0) lds[L_WSUM + w] = ls;
  }
  const int fr_row = 8 * r + ((lane >> 2) & 3), fr_col = (lane & 3) * 2;
  u32x4 wof[4];
#pragma unroll
  for (int s = 0; s < 4; ++s) wof[s] = u32x4{0u, 0u, 0u, 0u};
  if (half == 0) {
#pragma unroll
    for (int s = 0; s < 4; ++s)
      wof[s] = bfrag<64>(ldsu + L_S + (2 * s + (fr_row >> 4)) * 4096 + (fr_row & 15) * 64 + w * 8 + fr_col);
  }

  float acc[8];
#pragma unroll
  for (int j = 0; j < 8; ++j) acc[j] = 0.f;
  float Lb = 0.f;
  u64 gpm = 0, gpl = 0;
  gu64* pml = ws + WS_ML + (size_t)((b * NH + hh) * 2 + (half ^ 1)) * 2;
#define V_STEP(i) { const u32x4 vv = buf[32 + (i)]; const float pi = dpp<0x150 + ((i) & 15)>((i) < 16 ? p0 : p1); \
    _Pragma("unroll") for (int j = 0; j < 4; ++j) { acc[2 * j] = fmaf(pi, blo(vv[j]), acc[2 * j]); acc[2 * j + 1] = fmaf(pi, bhi(vv[j]), acc[2 * j + 1]); } \
    asm volatile("" : "+v"(acc[0]), "+v"(acc[1]), "+v"(acc[2]), "+v"(acc[3]), "+v"(acc[4]), "+v"(acc[5]), "+v"(acc[6]), "+v"(acc[7])); \
    ISSUE_AT(32 + (i)); SB(); if ((i) > 3 && (i) < 28 && ((i) % LOCK) == LOCK - 1) __builtin_amdgcn_s_barrier(); }
  V_STEP(0) V_STEP(1) V_STEP(2) V_STEP(3)
  LDS_BARRIER();
  {
    const f32x4 s0 = *reinterpret_cast<const f32x4*>(&lds[L_WSUM]), s1 = *reinterpret_cast<const f32x4*>(&lds[L_WSUM + 4]);
    Lb = ((s0[0] + s0[1]) + (s0[2] + s0[3])) + ((s1[0] + s1[1]) + (s1[2] + s1[3]));
    if (w == 0 && lane < 2) gstore(ws + WS_ML + (size_t)((b * NH + hh) * 2 + half) * 2 + lane, gpack(tag, lane == 0 ? Mb : Lb));
  }
  SB();
  V_STEP(4) V_STEP(5) V_STEP(6) V_STEP(7) V_STEP(8) V_STEP(9) V_STEP(10) V_STEP(11) V_STEP(12) V_STEP(13) V_STEP(14) V_STEP(15)
  gpm = gload(pml); gpl = gload(pml + 1);
  SB();
  V_STEP(16) V_STEP(17) V_STEP(18) V_STEP(19) V_STEP(20) V_STEP(21) V_STEP(22) V_STEP(23) V_STEP(24) V_STEP(25) V_STEP(26) V_STEP(27)
  static_assert(sched(63) <= 32 + 27, "all stream loads are issued before the tail loop");
  asm volatile("" : "+v"(gpm), "+v"(gpl));
#pragma clang loop unroll(disable)
  for (int spin = 0; spin < SPIN_CAP; ++spin) {
    if ((gbad(gpm, tag) | gbad(gpl, tag)) == 0u) break;
    gpm = gload(pml); gpl = gload(pml + 1);
  }
  float fsc;
  {
    const float m2 = gval(gpm), l2 = gval(gpl);
    const float M = fmaxf(Mb, m2), e = __builtin_amdgcn_exp2f((Mb - M) * LOG2E), e2 = __builtin_amdgcn_exp2f((m2 - M) * LOG2E);
    fsc = e * __builtin_amdgcn_rcpf(fmaf(Lb, e, l2 * e2));
  }
  const int mycol = 16 * hh + c;
  gu64* odst = ws + WS_O + ((size_t)(hh * 2 + half) * NB + b) * DH + tid;
  asm volatile("" : "+v"(odst), "+v"(fsc));
  SB();
  {
    V_STEP(28) V_STEP(29) V_STEP(30) V_STEP(31)
    {
      const float f = fsc;
      const float a0 = tr32(acc[0], acc[4]), a1 = tr32(acc[1], acc[5]), a2 = tr32(acc[2], acc[6]), a3 = tr32(acc[3], acc[7]);
      const float b0 = tr16(a0, a2) * f, b1 = tr16(a1, a3) * f;
      *reinterpret_cast<f32x2*>(&lds[L_PART + w * 128 + 8 * c + 4 * (r >> 1) + 2 * (r & 1)]) = f32x2{b0, b1};
    }
    LDS_BARRIER();
    if (w < 2) {
      float v[8];
#pragma unroll
      for (int i = 0; i < NW; ++i) v[i] = lds[L_PART + i * 128 + tid];
      const u64 og = gpack(tag, ((v[0] + v[1]) + (v[2] + v[3])) + ((v[4] + v[5]) + (v[6] + v[7])));
      gstore(odst, og);
    }
    SB();
    if (half == 0) {
      const u32 xraw = (u32)P.x[b * DM + mycol];
      gu64* ydst = ws + WS_Y + (((size_t)(r * 32 + 8 * b + w)) * NH + hh) * 16 + c;
      asm volatile("" : "+v"(ydst));
      {
        u64 g[2] = {0ull, 0ull};
        sweep<2, NB * DH>(ws + WS_O + (size_t)(hh * 2) * NB * DH + tid, g, tag);
        const u32 pk = pair_pack(rbf(gval(g[0]) + gval(g[1])));
        if ((lane & 1) == 0) ldsu[L_ACT + (tid >> 7) * P_O + ((tid & 127) >> 1)] = pk;
      }
      LDS_BARRIER();
      {
        u32x4 af[4];
#pragma unroll
        for (int s = 0; s < 4; ++s) af[s] = *reinterpret_cast<const u32x4*>(&ldsu[L_ACT + (lane & 3) * P_O + 16 * s + 4 * r]);
        SB();
        f32x4 d = mfma(af[0], wof[0], f32x4{0.f, 0.f, 0.f, 0.f}), d2 = mfma(af[1], wof[1], f32x4{0.f, 0.f, 0.f, 0.f});
        d = mfma(af[2], wof[2], d); d2 = mfma(af[3], wof[3], d2);
        d += d2;
        const u64 yg = gpack(tag, sel4(r, d[0], d[1], d[2], d[3]));
        gstore(ydst, yg);
      }
      if (w == 0) {
        u64 y[8];
        sweep<8, 64>(ws + WS_Y + (size_t)(b * 32 + hh) * (NH * 16) + lane, y, tag);
        float v = ((gval(y[0]) + gval(y[1])) + (gval(y[2]) + gval(y[3]))) + ((gval(y[4]) + gval(y[5])) + (gval(y[6]) + gval(y[7])));
        v = tr16(v, v); v = tr32(v, v);
        const float hval = rbf(blo(xraw) + rbf(v));
        if (lane < 16) gstore(ws + WS_H + b * DM + mycol, gpack(tag, hval));
        gu64* zp = ws + WS_Z + (size_t)(b * 32 + hh) * (NCG * 16);
        const bool live = lane < NCG * 16 - 640;
        u64 g[11];
        sweep<11, 64>(zp + lane, g, tag, zp + (live ? 640 + lane : lane));
        float z = (((gval(g[0]) + gval(g[1])) + (gval(g[2]) + gval(g[3]))) + ((gval(g[4]) + gval(g[5])) + (gval(g[6]) + gval(g[7])))) +
                  ((gval(g[8]) + gval(g[9])) + (live ? gval(g[10]) : 0.f));
        z = tr16(z, z); z = tr32(z, z);
        const float o = rbf(hval + rbf(z));
        {
          if (lane < 16) P.out[b * DM + mycol] = (unsigned short)(__float_as_uint(o) >> 16);
          if (bid == 0 && tid == 0) gstore(ws + WS_EPOCH, epoch + 1ull);
        }
      }
    } else if (is_ffn) {
      FFN_DMA_GU();
      asm volatile("s_waitcnt vmcnt(0)" ::: "memory");
      LDS_BARRIER();
      u32x4 guf[16], wdf[4];
      const int gcol = ((lane & 2) ? 16 : 0) + (w & 3) * 4 + (lane & 1) * 2;
#pragma unroll
      for (int s = 0; s < 16; ++s) guf[s] = bfrag<32>(ldsu + L_S + (s >> 1) * 4096 + (s & 1) * 1024 + fr_row * 32 + gcol);
#pragma unroll
      for (int t = 0; t < 4; ++t) wdf[t] = bfrag<32>(ldsu + L_S + w * 4096 + 2048 + fr_row * 32 + t * 8 + fr_col);
      gu64* zdst = ws + WS_Z + (((size_t)(4 * w + r)) * NCG + cg) * 16 + c;
      asm volatile("" : "+v"(zdst));
      if (w < 4) {
        u64 g[8];
        sweep<8, 128, 2>(ws + WS_H + w * DM + 2 * lane, g, tag);
        float h[8];
#pragma unroll
        for (int i = 0; i < 8; ++i) h[i] = gval(g[i]);
        const float mean = wave_sum(((h[0] + h[1]) + (h[2] + h[3])) + ((h[4] + h[5]) + (h[6] + h[7]))) * (1.f / DM);
        float q0 = 0.f, q1 = 0.f;
#pragma unroll
        for (int i = 0; i < 4; ++i) { const float d0 = h[i] - mean, d1 = h[i + 4] - mean; q0 = fmaf(d0, d0, q0); q1 = fmaf(d1, d1, q1); }
        const float mb = rbf(mean);
        u32 dk[4];
#pragma unroll
        for (int j = 0; j < 4; ++j) dk[j] = pk2(h[2 * j] - mb, h[2 * j + 1] - mb);
        const float var = rbf(wave_sum(q0 + q1) * (1.f / DM));
        const float inv = __builtin_amdgcn_rcpf(rbf(__builtin_amdgcn_sqrtf(rbf(var + EPS))));
#pragma unroll
        for (int j = 0; j < 4; ++j) ldsu[L_HN + w * P_HN + 64 * j + lane] = pk2(blo(dk[j]) * inv, bhi(dk[j]) * inv);
      }
      LDS_BARRIER();
      if (w < 4) {
        u32x4 af[16];
#pragma unroll
        for (int s = 0; s < 16; ++s) af[s] = *reinterpret_cast<const u32x4*>(&ldsu[L_HN + (lane & 3) * P_HN + 16 * s + 4 * r]);
        SB();
        f32x4 d0 = {0.f, 0.f, 0.f, 0.f}, d1 = {0.f, 0.f, 0.f, 0.f};
#pragma unroll
        for (int s = 0; s < 16; s += 2) { d0 = mfma(af[s], guf[s], d0); d1 = mfma(af[s + 1], guf[s + 1], d1); }
        const float v = rbf(sel4(r, d0[0] + d1[0], d0[1] + d1[1], d0[2] + d1[2], d0[3] + d1[3]));
        const float uu = dpp<ROR8>(v);
        const float sg = rbf(v * __builtin_amdgcn_rcpf(1.f + __builtin_amdgcn_exp2f(-v * LOG2E)));
        const u32 pk = pair_pack(rbf(sg * uu));
        if (c < 8 && (lane & 1) == 0) ldsu[L_A2 + r * 16 + 4 * w + (c >> 1)] = pk;
      }
      LDS_BARRIER();
      {
        const u32x4 a = *reinterpret_cast<const u32x4*>(&ldsu[L_A2 + (lane & 3) * 16 + 4 * r]);
        f32x4 d[4];
#pragma unroll
        for (int t = 0; t < 4; ++t) d[t] = mfma(a, wdf[t], f32x4{0.f, 0.f, 0.f, 0.f});
#pragma unroll
        for (int i = 0; i < 4; ++i) {
          const u64 zg = gpack(tag, sel4(r, d[0][i], d[1][i], d[2][i], d[3][i]));
          gstore(zdst + (size_t)i * (32 * NCG * 16), zg);
        }
      }
    }
  }
#undef V_STEP
}

extern "C" void kernel_launch(void* const* d_in, const int* in_sizes, int n_in, void* d_out, int out_size, void* d_ws, size_t ws_size,
                              hipStream_t stream) {
  Params P;
  P.x = (const unsigned short*)d_in[0]; P.kc = (const unsigned short*)d_in[1]; P.vc = (const unsigned short*)d_in[2];
  P.wq = (const unsigned short*)d_in[3]; P.wo = (const unsigned short*)d_in[4];
  P.wg = (const unsigned short*)d_in[6]; P.wu = (const unsigned short*)d_in[7]; P.wd = (const unsigned short*)d_in[8];
  P.out = (unsigned short*)d_out; P.ws = (u64*)d_ws;
  hipLaunchKernelGGL(fused_decode_block, dim3(256), dim3(NTHR), 0, stream, P);
}
```
